```python
import torch

def _softmax(x, axis=-1):
    return torch.softmax(x, dim=axis)
def _gelu(x):
    return torch.nn.functional.gelu(x, approximate="tanh")
def _silu(x):
    return torch.nn.functional.silu(x)
def _sigmoid(x):
    return torch.sigmoid(x)
def _layernorm(x, axis=-1, eps=1e-5):
    m = x.mean(dim=axis, keepdim=True)
    v = x.var(dim=axis, keepdim=True, unbiased=False)
    return (x - m) / torch.sqrt(v + eps)
def _scatter_add(tgt, idx, src, axis=0):
    return tgt.clone().index_add_(axis, idx, src)
def _segment_sum(src, seg, n):
    out = torch.zeros((n,) + src.shape[1:], dtype=src.dtype)
    return out.index_add_(0, seg, src)
def _pad(x, pw):
    flat = tuple(v for pair in reversed(pw) for v in pair)
    return torch.nn.functional.pad(x, flat)

kernel_name = 'standalone__normalfloat_lut_codebook_lookup__prompt'

def setup_inputs():

    _w = (lambda a: (lambda t: t.to(torch.bfloat16) if t.is_floating_point() else t)(torch.as_tensor(a)))
    B, S, D, F = 1, 2048, 1024, 3584
    G = 128 if 128 > 0 else D
    D = (D // G) * G
    ng = D // G
    nf4 = torch.tensor([
        -1.0, -0.6961928009986877, -0.5250730514526367, -0.39491748809814453,
        -0.28444138169288635, -0.18477343022823334, -0.09105003625154495, 0.0,
        0.07958029955625534, 0.16093020141124725, 0.24611230194568634, 0.33791524171829224,
        0.44070982933044434, 0.5626170039176941, 0.7229568362236023, 1.0
    ], dtype=float)
    qw = torch.randint(0, 16, (D, F)).to(torch.int64)
    absmax = (torch.abs((lambda *s: torch.randn(*s, dtype=torch.bfloat16) * 0.05)(ng, F)) + 0.01).to(float)
    return {
        "x": _w((lambda *s: torch.randn(*s, dtype=torch.bfloat16) * 0.05)(B, S, D)),
        "qw": _w(qw),
        "codebook": _w(nf4),
        "absmax": _w(absmax),
    }

def reference(x, qw, codebook, absmax):
    B, S, D = x.shape
    ng, F = absmax.shape
    G = D // ng
    w_nf = codebook[qw]
    s = (lambda x, n, axis: torch.repeat_interleave(x, n, dim=axis))(absmax, G, axis=0)
    w = (lambda x, dt: x.to(dt))(w_nf * s, x.dtype)
    return torch.einsum("bsd,df->bsf", x, w)
```

```cpp
#include <hip/hip_runtime.h>
#include <hip/hip_bf16.h>
#include <stdint.h>

using bf16x8 = __attribute__((ext_vector_type(8))) short;
using f32x4 = __attribute__((ext_vector_type(4))) float;

constexpr int M = 2048, K = 1024, N = 3584, G = 128;

__device__ __forceinline__ float bf2f(unsigned short h) { return __uint_as_float(((unsigned)h) << 16); }
__device__ __forceinline__ unsigned short f2bf(float f) {
  __hip_bfloat16 b = __float2bfloat16(f);
  return *reinterpret_cast<unsigned short*>(&b);
}

__global__ void dequant_kernel(const int* __restrict__ qw, const unsigned short* __restrict__ cb,
                               const unsigned short* __restrict__ absmax, unsigned short* __restrict__ Wt) {
  __shared__ unsigned short tile[64][66];
  int n0 = blockIdx.x * 64, k0 = blockIdx.y * 64;
  int tx = threadIdx.x & 63, ty = threadIdx.x >> 6;
  for (int r = ty; r < 64; r += 4) {
    int k = k0 + r, n = n0 + tx;
    int q = qw[k * N + n];
    float w = bf2f(cb[q]) * bf2f(absmax[(k / G) * N + n]);
    tile[r][tx] = f2bf(w);
  }
  __syncthreads();
  for (int r = ty; r < 64; r += 4) {
    Wt[(n0 + r) * K + k0 + tx] = tile[tx][r];
  }
}

__global__ __launch_bounds__(256) void gemm_kernel(const unsigned short* __restrict__ A, const unsigned short* __restrict__ Bt,
                                                   unsigned short* __restrict__ C) {
  int wid = threadIdx.x >> 6, lane = threadIdx.x & 63;
  int wr = wid >> 1, wc = wid & 1;
  int brow = blockIdx.y * 64 + wr * 32, bcol = blockIdx.x * 64 + wc * 32;
  int fr = lane & 15, fq = lane >> 4;
  f32x4 acc[2][2] = {};
  for (int k = 0; k < K; k += 32) {
    bf16x8 a[2], b[2];
    for (int i = 0; i < 2; ++i) a[i] = *reinterpret_cast<const bf16x8*>(A + (brow + i * 16 + fr) * K + k + fq * 8);
    for (int i = 0; i < 2; ++i) b[i] = *reinterpret_cast<const bf16x8*>(Bt + (bcol + i * 16 + fr) * K + k + fq * 8);
    for (int i = 0; i < 2; ++i)
      for (int j = 0; j < 2; ++j) acc[i][j] = __builtin_amdgcn_mfma_f32_16x16x32_bf16(a[i], b[j], acc[i][j], 0, 0, 0);
  }
  for (int i = 0; i < 2; ++i)
    for (int j = 0; j < 2; ++j)
      for (int r = 0; r < 4; ++r) C[(brow + i * 16 + fq * 4 + r) * N + bcol + j * 16 + fr] = f2bf(acc[i][j][r]);
}

extern "C" void kernel_launch(void* const* d_in, const int* in_sizes, int n_in, void* d_out, int out_size, void* d_ws,
                              size_t ws_size, hipStream_t stream) {
  const unsigned short* x = (const unsigned short*)d_in[0];
  const int* qw = (const int*)d_in[1];
  const unsigned short* cb = (const unsigned short*)d_in[2];
  const unsigned short* absmax = (const unsigned short*)d_in[3];
  unsigned short* out = (unsigned short*)d_out;
  unsigned short* Wt = (unsigned short*)d_ws;
  dequant_kernel<<<dim3(N / 64, K / 64), 256, 0, stream>>>(qw, cb, absmax, Wt);
  gemm_kernel<<<dim3(N / 64, M / 64), 256, 0, stream>>>(x, Wt, out);
}
```

```cpp
#include <hip/hip_runtime.h>
#include <hip/hip_bf16.h>
#include <stdint.h>

typedef short bf16x8 __attribute__((ext_vector_type(8)));
typedef float f32x4 __attribute__((ext_vector_type(4)));
typedef int i32x4 __attribute__((ext_vector_type(4)));
typedef unsigned u32x4 __attribute__((ext_vector_type(4)));
typedef unsigned u32x2 __attribute__((ext_vector_type(2)));
#define LAS __attribute__((address_space(3)))

constexpr int M_ = 2048, K_ = 1024, N_ = 3584;
constexpr int BM = 128, BN = 224;
constexpr int XSLOT = 16384, WSLOT = 28672, NSLOT = 3;
constexpr int LDS_X = 0, LDS_W = NSLOT * XSLOT;
constexpr int LDS_BYTES = NSLOT * (XSLOT + WSLOT);

__device__ __forceinline__ float bf2f(unsigned short h) { return __uint_as_float(((unsigned)h) << 16); }
typedef __bf16 bf16x2_t __attribute__((ext_vector_type(2)));
typedef float f32x2 __attribute__((ext_vector_type(2)));
__device__ __forceinline__ unsigned cvt_pk_bf16(float lo, float hi) {
  const bf16x2_t v = __builtin_convertvector(f32x2{lo, hi}, bf16x2_t);
  return __builtin_bit_cast(unsigned, v);
}
__device__ __forceinline__ i32x4 make_srd(const void* p, unsigned bytes) {
  unsigned long long a = (unsigned long long)p;
  i32x4 r;
  r.x = __builtin_amdgcn_readfirstlane((int)(unsigned)a);
  r.y = __builtin_amdgcn_readfirstlane((int)(unsigned)(a >> 32));
  r.z = __builtin_amdgcn_readfirstlane((int)bytes);
  r.w = 0x00020000;
  return r;
}

__global__ __launch_bounds__(448) void k_dequant(const int* __restrict__ qw, const unsigned short* __restrict__ cb,
                                                 const unsigned short* __restrict__ absmax, unsigned short* __restrict__ wimg) {
  const int lane = threadIdx.x & 63;
  const int task = blockIdx.x * 7 + (threadIdx.x >> 6);
  const int kg = task / 14, ch = task % 14;
  const int n0 = ch * 256 + lane * 4;
  i32x4 q[8];
#pragma unroll
  for (int r = 0; r < 8; ++r) q[r] = __builtin_nontemporal_load((const i32x4*)(qw + (size_t)(kg * 8 + r) * N_ + n0));
  const float tab = bf2f(cb[lane & 15]);
  const u32x2 sraw = *(const u32x2*)(absmax + (kg >> 4) * N_ + n0);
  float s[4] = {__uint_as_float(sraw.x << 16), __uint_as_float(sraw.x & 0xffff0000u), __uint_as_float(sraw.y << 16), __uint_as_float(sraw.y & 0xffff0000u)};
  const int j = n0 / BN, nn = n0 % BN, wc = nn / 112, m112 = nn % 112;
  int tile, slot;
  if (m112 < 96) { const int p = m112 >> 5, cc = m112 & 31; tile = 7 * wc + 2 * p + ((cc >> 2) & 1); slot = 4 * (cc >> 3); }
  else { tile = 7 * wc + 6; slot = m112 - 96; }
  const int st = kg >> 2, fq = kg & 3;
  u32x4* dst = (u32x4*)((char*)wimg + ((size_t)(j * 32 + st) * 14 + tile) * 1024 + (fq * 16 + slot) * 16);
#pragma unroll
  for (int c = 0; c < 4; ++c) {
    float w[8];
#pragma unroll
    for (int r = 0; r < 8; ++r) {
      const int qi = q[r][c];
      const float cv = __int_as_float(__builtin_amdgcn_ds_bpermute(qi << 2, __float_as_int(tab)));
      w[r] = cv * s[c];
    }
    u32x4 o;
    o.x = cvt_pk_bf16(w[0], w[1]); o.y = cvt_pk_bf16(w[2], w[3]); o.z = cvt_pk_bf16(w[4], w[5]); o.w = cvt_pk_bf16(w[6], w[7]);
    dst[c] = o;
  }
}

#define DMA16(voff, srd, soff, imm) asm volatile("buffer_load_dwordx4 %0, %1, %2 offen offset:" #imm " lds" ::"v"(voff), "s"(srd), "s"(soff) : "memory")
#define SETM0(val) asm volatile("s_mov_b32 m0, %0\n\ts_nop 0" ::"s"(val) : "memory")
#define WAITV(n) asm volatile("s_waitcnt vmcnt(" #n ")" ::: "memory")
#define BAR() do { asm volatile("" ::: "memory"); __builtin_amdgcn_s_barrier(); asm volatile("" ::: "memory"); } while (0)

__global__ __launch_bounds__(512, 2) void k_gemm(const unsigned short* __restrict__ x, const unsigned short* __restrict__ wimg,
                                                 unsigned short* __restrict__ out
) {
  extern __shared__ __attribute__((aligned(16))) char smem[];
  const int bid = blockIdx.x, xcd = bid & 7, idx = bid >> 3;
  const int mb = (xcd & 1) * 8 + (idx & 7), jb = (xcd >> 1) * 4 + (idx >> 3);
  const int tid = threadIdx.x, lane = tid & 63;
  const int wid = __builtin_amdgcn_readfirstlane(tid >> 6);
  constexpr int NS = K_ / 64;
  if (wid >= 4) {
    const int L = wid - 4;
    const i32x4 srdx = make_srd(x, (unsigned)(M_ * K_ * 2));
    const i32x4 srdw = make_srd(wimg, (unsigned)(K_ * N_ * 2));
    unsigned vx[4];
#pragma unroll
    for (int i = 0; i < 4; ++i) {
      const int row = L * 32 + i * 8 + (lane >> 3);
      const int c = (lane & 7) ^ ((row >> 1) & 7);
      vx[i] = (unsigned)((mb * BM + row) * (K_ * 2) + c * 16 - i * 1024);
    }
    const unsigned vw = (unsigned)(lane * 16);
    const unsigned wbase = (unsigned)(jb * 32 * 14336 + L * 7 * 1024);
    auto issue = [&](int s, int slot) {
      const unsigned sx = (unsigned)(s * 128);
      const unsigned mx = (unsigned)(LDS_X + slot * XSLOT + L * 4096);
      SETM0(mx);
      DMA16(vx[0], srdx, sx, 0); DMA16(vx[1], srdx, sx, 1024); DMA16(vx[2], srdx, sx, 2048); DMA16(vx[3], srdx, sx, 3072);
      const unsigned sw = wbase + (unsigned)(s * WSLOT);
      const unsigned mw = (unsigned)(LDS_W + slot * WSLOT + L * 7168);
      SETM0(mw);
      DMA16(vw, srdw, sw, 0); DMA16(vw, srdw, sw, 1024); DMA16(vw, srdw, sw, 2048); DMA16(vw, srdw, sw, 3072);
      const unsigned sw2 = sw + 4096, mw2 = mw + 4096;
      SETM0(mw2);
      DMA16(vw, srdw, sw2, 0); DMA16(vw, srdw, sw2, 1024); DMA16(vw, srdw, sw2, 2048);
    };
    issue(0, 0); issue(1, 1); issue(2, 2);
    WAITV(22);
    BAR();
    int slot = 0;
    for (int s = 0; s < NS - 1; ++s) {
      if (s < NS - 2) WAITV(11); else WAITV(0);
      BAR();
      if (s + 3 < NS) issue(s + 3, slot);
      slot = (slot == NSLOT - 1) ? 0 : slot + 1;
    }
  } else {
    const int wr = wid >> 1, wc = wid & 1;
    const int fr = lane & 15, fq = lane >> 4;
    const int f = (fr >> 1) & 7;
    const int xa0 = LDS_X + wr * 64 * 128 + (fr >> 3) * 1024 + (fr & 7) * 128 + ((fq ^ f) * 16);
    const int xa1 = xa0 ^ 64;
    const int wa = LDS_W + wc * 7 * 1024 + lane * 16;
    f32x4 acc[4][7];
#pragma unroll
    for (int m = 0; m < 4; ++m)
#pragma unroll
      for (int t = 0; t < 7; ++t) { acc[m][t] = f32x4{0.f, 0.f, 0.f, 0.f}; asm volatile("" : "+v"(acc[m][t])); }
    bf16x8 xf0[4], wf0[7], xf1[4], wf1[7];
    LAS char* lds = (LAS char*)smem;
#define RD(p) (*(const LAS bf16x8*)(lds + (p)))
#define LOADF(xf, wf, xa, wofs) do { _Pragma("unroll") for (int t = 0; t < 7; ++t) wf[t] = RD((wofs) + t * 1024); \
                                     _Pragma("unroll") for (int m = 0; m < 4; ++m) xf[m] = RD((xa) + m * 2048); } while (0)
#define MMA(xf, wf) do { _Pragma("unroll") for (int m = 0; m < 4; ++m) _Pragma("unroll") for (int t = 0; t < 7; ++t) \
      acc[m][t] = __builtin_amdgcn_mfma_f32_16x16x32_bf16(wf[t], xf[m], acc[m][t], 0, 0, 0); } while (0)
#define INTERLEAVE() do { _Pragma("unroll") for (int i = 0; i < 11; ++i) { __builtin_amdgcn_sched_group_barrier(0x8, 1, 0); __builtin_amdgcn_sched_group_barrier(0x100, 1, 0); } \
      __builtin_amdgcn_sched_group_barrier(0x8, 17, 0); } while (0)
    BAR();
    LOADF(xf0, wf0, xa0, wa);
    int xo = 0, wo = 0;
    for (int s = 0; s < NS - 1; ++s) {
      LOADF(xf1, wf1, xa1 + xo, wa + wo + 14336);
      MMA(xf0, wf0);
      INTERLEAVE();
      __builtin_amdgcn_sched_barrier(0);
      asm volatile("s_waitcnt lgkmcnt(0)" ::: "memory");
      BAR();
      __builtin_amdgcn_sched_barrier(0);
      xo = (xo == (NSLOT - 1) * XSLOT) ? 0 : xo + XSLOT;
      wo = (wo == (NSLOT - 1) * WSLOT) ? 0 : wo + WSLOT;
      LOADF(xf0, wf0, xa0 + xo, wa + wo);
      MMA(xf1, wf1);
      INTERLEAVE();
      __builtin_amdgcn_sched_barrier(0);
    }
    LOADF(xf1, wf1, xa1 + xo, wa + wo + 14336);
    MMA(xf0, wf0);
    INTERLEAVE();
    MMA(xf1, wf1);
    unsigned short* orow = out + (size_t)(mb * BM + wr * 64 + fr) * N_ + jb * BN + wc * 112;
#pragma unroll
    for (int m = 0; m < 4; ++m) {
      unsigned short* p = orow + (size_t)m * 16 * N_;
#pragma unroll
      for (int pp = 0; pp < 3; ++pp) {
        u32x4 o;
        o.x = cvt_pk_bf16(acc[m][2 * pp][0], acc[m][2 * pp][1]); o.y = cvt_pk_bf16(acc[m][2 * pp][2], acc[m][2 * pp][3]);
        o.z = cvt_pk_bf16(acc[m][2 * pp + 1][0], acc[m][2 * pp + 1][1]); o.w = cvt_pk_bf16(acc[m][2 * pp + 1][2], acc[m][2 * pp + 1][3]);
        *(u32x4*)(p + pp * 32 + fq * 8) = o;
      }
      u32x2 o2;
      o2.x = cvt_pk_bf16(acc[m][6][0], acc[m][6][1]); o2.y = cvt_pk_bf16(acc[m][6][2], acc[m][6][3]);
      *(u32x2*)(p + 96 + fq * 4) = o2;
    }
  }
}

extern "C" void kernel_launch(void* const* d_in, const int* in_sizes, int n_in, void* d_out, int out_size, void* d_ws,
                              size_t ws_size, hipStream_t stream) {
  const unsigned short* x = (const unsigned short*)d_in[0];
  const int* qw = (const int*)d_in[1];
  const unsigned short* cb = (const unsigned short*)d_in[2];
  const unsigned short* absmax = (const unsigned short*)d_in[3];
  unsigned short* out = (unsigned short*)d_out;
  unsigned short* wimg = (unsigned short*)d_ws;
  (void)hipFuncSetAttribute((const void*)k_gemm, hipFuncAttributeMaxDynamicSharedMemorySize, LDS_BYTES);
  k_dequant<<<256, 448, 0, stream>>>(qw, cb, absmax, wimg);
  k_gemm<<<256, 512, LDS_BYTES, stream>>>(x, wimg, out
  );
}
```

```cpp
#include <hip/hip_runtime.h>
#include <hip/hip_bf16.h>
#include <stdint.h>

typedef short bf16x8 __attribute__((ext_vector_type(8)));
typedef float f32x4 __attribute__((ext_vector_type(4)));
typedef float f32x2 __attribute__((ext_vector_type(2)));
typedef __bf16 bf16x2_t __attribute__((ext_vector_type(2)));
typedef int i32x4 __attribute__((ext_vector_type(4)));
typedef unsigned u32x4 __attribute__((ext_vector_type(4)));
typedef unsigned u32x2 __attribute__((ext_vector_type(2)));
#define LAS __attribute__((address_space(3)))

#define XMAP 1
#define ORDER 1
#define EPI 1
#define STPOL 0
constexpr int M_ = 2048, K_ = 1024, N_ = 3584;
constexpr int BM = 256, BN = 112, NS = K_ / 64;
constexpr int XSLOT = 32768, WSLOT = 14336, TAB_G = 3584;
constexpr int LDS_X = 0, LDS_W = 3 * XSLOT, LDS_T = LDS_W + 2 * WSLOT;
constexpr int LDS_D = LDS_T + 8 * TAB_G;
constexpr int LDS_BYTES = LDS_D + 1024;

__device__ __forceinline__ unsigned cvt_pk_bf16(float lo, float hi) {
  const bf16x2_t v = __builtin_convertvector(f32x2{lo, hi}, bf16x2_t);
  return __builtin_bit_cast(unsigned, v);
}
__device__ __forceinline__ i32x4 make_srd(const void* p, unsigned bytes) {
  unsigned long long a = (unsigned long long)p;
  i32x4 r;
  r.x = __builtin_amdgcn_readfirstlane((int)(unsigned)a);
  r.y = __builtin_amdgcn_readfirstlane((int)(unsigned)(a >> 32));
  r.z = __builtin_amdgcn_readfirstlane((int)bytes);
  r.w = 0x00020000;
  return r;
}
__device__ __forceinline__ unsigned perm(unsigned s0, unsigned s1, unsigned sel) { return __builtin_amdgcn_perm(s0, s1, sel); }

#define STAMP(i)

#define DMA16(voff, srd, soff, imm) asm volatile("buffer_load_dwordx4 %0, %1, %2 offen offset:" #imm " lds" ::"v"(voff), "s"(srd), "s"(soff) : "memory")
#define QLOAD(dst, voff, srd, soff) asm volatile("buffer_load_dwordx4 %0, %1, %2, %3 offen" : "=v"(dst) : "v"(voff), "s"(srd), "s"(soff))
#define SETM0(val) asm volatile("s_mov_b32 m0, %0\n\ts_nop 0" ::"s"(val) : "memory")
#define XREAL(c) (c)
#define QREAL(c) (c)
#define WAITV(n) asm volatile("s_waitcnt vmcnt(" #n ")" ::: "memory")
#define WAITRAW(n, R) WAITRAW_(n, R)
#define WAITRAW_(n, R) asm volatile("s_waitcnt vmcnt(" #n ")" : "+v"(R[0]), "+v"(R[1]), "+v"(R[2]), "+v"(R[3]), "+v"(R[4]), "+v"(R[5]), "+v"(R[6]), "+v"(R[7]))
#define BAR() do { asm volatile("" ::: "memory"); __builtin_amdgcn_s_barrier(); asm volatile("" ::: "memory"); } while (0)
#define LBAR() BAR()
#define SCHED() __builtin_amdgcn_sched_barrier(0)

__global__ __launch_bounds__(512, 2) void k_fused(const unsigned short* __restrict__ x, const int* __restrict__ qw,
                                                  const unsigned short* __restrict__ cb, const unsigned short* __restrict__ absmax,
                                                  unsigned short* __restrict__ out
) {
  extern __shared__ __attribute__((aligned(16))) char smem[];
  LAS char* lds = (LAS char*)smem;
  const int bid = blockIdx.x, xcd = bid & 7, idx = bid >> 3;
  const int mb = (xcd & 1) * 4 + (idx & 3), jb = (xcd >> 1) * 8 + (idx >> 2);
  const int tid = threadIdx.x, lane = tid & 63;
  const int wid = __builtin_amdgcn_readfirstlane(tid >> 6);
  STAMP(wid >= 4 ? 8 : 0);
  if (wid >= 4) {
    const int L = wid - 4;
    const i32x4 srdx = make_srd(x, (unsigned)(M_ * K_ * 2));
    const i32x4 srdq = make_srd(qw, (unsigned)(K_ * N_ * 4));
    i32x4 srdx0 = srdx, srdq0 = srdq; srdx0.z = 0; srdq0.z = 0;
    unsigned vx[4];
#pragma unroll
    for (int i = 0; i < 4; ++i) {
      const int row = L * 64 + i * 8 + (lane >> 3);
      const int c = (lane & 7) ^ ((row >> 1) & 7);
      vx[i] = (unsigned)((mb * BM + row) * (K_ * 2) + c * 16 - i * 1024);
    }
    const int c4 = lane & 31, kg = 2 * L + (lane >> 5);
    const bool act = c4 < 28;
    const unsigned vq = (unsigned)((kg * 8 * N_ + jb * BN + c4 * 4) * 4) | ((unsigned)(!act) << 31);
    int tile, a;
    if (c4 < 24) { tile = 2 * (c4 >> 3) + (c4 & 1); a = (c4 & 7) >> 1; } else { tile = 6; a = c4 & 3; }
    const int v2 = (tile & 1) + 2 * (a >> 1);
    const int wdst = LDS_W + (kg >> 2) * 7168 + tile * 1024 + (kg & 3) * 256 + a * 64;
    int wd[2][4];
#pragma unroll
    for (int i = 0; i < 4; ++i) { wd[0][i] = act ? wdst + ((i ^ v2) * 16) : LDS_D + lane * 16; wd[1][i] = act ? wdst + WSLOT + ((i ^ v2) * 16) : LDS_D + lane * 16; }
    const int tadr = LDS_T + c4 * 16;
    const unsigned long long qmask = 0x0fffffff0fffffffull;

    unsigned vqr[8];
#pragma unroll
    for (int r = 0; r < 8; ++r) vqr[r] = vq + r * (N_ * 4);
    i32x4 rawA[8], rawB[8];
#define XPIECE(j, srd, sx_, sx2_) do { if (((j) & 3) == 0) DMA16(vx[0], srd, ((j) < 4 ? sx_ : sx2_), 0); if (((j) & 3) == 1) DMA16(vx[1], srd, ((j) < 4 ? sx_ : sx2_), 1024); \
                   if (((j) & 3) == 2) DMA16(vx[2], srd, ((j) < 4 ? sx_ : sx2_), 2048); if (((j) & 3) == 3) DMA16(vx[3], srd, ((j) < 4 ? sx_ : sx2_), 3072); } while (0)
    {
      const unsigned mx = (unsigned)(LDS_X + L * 8192), mx2 = mx + 4096, z = 0, z2 = 32 * K_ * 2;
      SETM0(mx); XPIECE(0, srdx, z, z2); XPIECE(1, srdx, z, z2); XPIECE(2, srdx, z, z2); XPIECE(3, srdx, z, z2);
      SETM0(mx2); XPIECE(4, srdx, z, z2); XPIECE(5, srdx, z, z2); XPIECE(6, srdx, z, z2); XPIECE(7, srdx, z, z2);
#pragma unroll
      for (int r = 0; r < 8; ++r) QLOAD(rawA[r], vqr[r], srdq, z);
      const unsigned s1 = 64 * N_ * 4;
#pragma unroll
      for (int r = 0; r < 8; ++r) QLOAD(rawB[r], vqr[r], srdq, s1);
    }
    BAR();
    WAITV(8);
    STAMP(9);
    unsigned xm = (unsigned)(LDS_X + XSLOT + L * 8192);
    unsigned sx = 128, sq = 2 * 64 * N_ * 4;
    i32x4 srdX = srdx, srdQ = srdq;
#define TS(i)
#define PK(i) do { pk[i][0] = perm(R_[1][i], R_[0][i], 0x0c0c0400u) | perm(R_[3][i], R_[2][i], 0x04000c0cu); \
                   pk[i][1] = perm(R_[5][i], R_[4][i], 0x0c0c0400u) | perm(R_[7][i], R_[6][i], 0x04000c0cu); } while (0)
#define LA(i, h) do { c1 = pk[i][h] ^ 0x08080808u; lo = perm(tl[i][1], tl[i][0], pk[i][h]) ^ perm(tl[i][3], tl[i][2], c1); } while (0)
#define LB(i, h) do { const unsigned hi = perm(th[i][1], th[i][0], pk[i][h]) ^ perm(th[i][3], th[i][2], c1); \
                      o[2 * h] = perm(hi, lo, 0x05010400u); o[2 * h + 1] = perm(hi, lo, 0x07030602u); } while (0)
#define WR(i) do { *(LAS u32x4*)(lds + wd[wsel_][i]) = o; } while (0)
#define XD(j) do { SCHED(); if ((j) == 4) SETM0(xm + 4096); XPIECE(j, srdX, sx, sx + 32 * K_ * 2); SCHED(); } while (0)
#define QD(r) do { SCHED(); QLOAD(R_[r], vqr[r], srdQ, sq); SCHED(); } while (0)
#define TAILSC1 SCHED(); const unsigned sxn = sx + 128, sqn = sq + 64 * N_ * 4; \
      const unsigned xmn = (xm >= (unsigned)(LDS_X + 2 * XSLOT)) ? xm - 2 * XSLOT : xm + XSLOT;
#define WR_N 18
#define WAIT_X() WAITV(19)
#define ORDER_BODY \
      PK(0); PK(1); XD(2); PK(2); PK(3); QD(0); TS(2); \
      LA(0, 0); LB(0, 0); XD(3); LA(0, 1); QD(1); LB(0, 1); WR(0); \
      XD(4); LA(1, 0); LB(1, 0); QD(2); LA(1, 1); XD(5); LB(1, 1); WR(1); \
      QD(3); LA(2, 0); LB(2, 0); XD(6); LA(2, 1); QD(4); LB(2, 1); WR(2); \
      XD(7); LA(3, 0); LB(3, 0); QD(5); LA(3, 1); LB(3, 1); WR(3); \
      TAILSC1 QD(6); QD(7); \
      xm = xmn; srdX.z = XREAL(sxn < NS * 128) ? srdx.z : 0; srdQ.z = QREAL(sqn < (unsigned)NS * 64 * N_ * 4) ? srdq.z : 0; sx = sxn; sq = sqn;
#define ITER(R, wsel) do { \
      i32x4 (&R_)[8] = R; constexpr int wsel_ = wsel; \
      unsigned pk[4][2], c1, lo; u32x4 o; \
      XD(0); XD(1); \
      if (wsel == 0) { asm volatile("" : "+v"(tcur)); \
        _Pragma("unroll") for (int i = 0; i < 4; ++i) { tl[i] = *(const LAS u32x4*)(lds + tcur + (2 * i) * 448); th[i] = *(const LAS u32x4*)(lds + tcur + (2 * i + 1) * 448); } \
        tcur += TAB_G; SCHED(); } \
      TS(0); WAITRAW(WR_N, R_); TS(1); \
      ORDER_BODY \
      SETM0(xm); \
      TS(3); WAIT_X(); TS(4); \
      asm volatile("s_waitcnt lgkmcnt(0)" ::: "memory"); TS(5); \
      LBAR(); TS(6); } while (0)
    SETM0(xm);
    int tcur = tadr;
#pragma unroll 1
    for (int g = 0; g < 8; ++g) {
      u32x4 tl[4], th[4];
      ITER(rawA, 0);
      ITER(rawB, 1);
    }
    WAITV(0);
    STAMP(10);
  } else {
    {
      const u32x4 cbr0 = *(const u32x4*)cb, cbr1 = *(const u32x4*)(cb + 8);
      float cf[16];
#pragma unroll
      for (int j = 0; j < 4; ++j) { cf[2 * j] = __uint_as_float(cbr0[j] << 16); cf[2 * j + 1] = __uint_as_float(cbr0[j] & 0xffff0000u);
                                    cf[8 + 2 * j] = __uint_as_float(cbr1[j] << 16); cf[8 + 2 * j + 1] = __uint_as_float(cbr1[j] & 0xffff0000u); }
      unsigned short am[4];
#pragma unroll
      for (int i = 0; i < 4; ++i) { const int p = tid + 256 * i; const int pp = p < 896 ? p : 0; am[i] = absmax[(pp / 112) * N_ + jb * BN + pp % 112]; }
      auto plane = [](u32x4 t) {
        const unsigned a1 = t[1] ^ 0xFFFFFF00u, b1 = t[3] ^ 0xFFFFFF00u;
        unsigned a0 = t[0], b0 = t[2];
#pragma unroll
        for (int it = 0; it < 3; ++it) { const unsigned na = t[0] ^ perm(b1, b0, 0x0B0A0908u), nb = t[2] ^ perm(a1, a0, 0x0B0A0908u); a0 = na; b0 = nb; }
        return u32x4{a0, a1, b0, b1};
      };
#pragma unroll
      for (int i = 0; i < 4; ++i) {
        const int p = tid + 256 * i;
        const float s = __uint_as_float(((unsigned)am[i]) << 16);
        unsigned d[8];
#pragma unroll
        for (int j = 0; j < 8; ++j) d[j] = cvt_pk_bf16(cf[2 * j] * s, cf[2 * j + 1] * s);
        u32x4 lo, hi;
#pragma unroll
        for (int j = 0; j < 4; ++j) { lo[j] = perm(d[2 * j + 1], d[2 * j], 0x06040200u); hi[j] = perm(d[2 * j + 1], d[2 * j], 0x07050301u); }
        lo = plane(lo); hi = plane(hi);
        if (p < 896) {
          const int g = p / 112, n = p % 112;
          const int ta = LDS_T + g * TAB_G + (n & 3) * 2 * 448 + (n >> 2) * 16;
          *(LAS u32x4*)(lds + ta) = lo;
          *(LAS u32x4*)(lds + ta + 448) = hi;
        }
        if (i == 0) { asm volatile("s_waitcnt lgkmcnt(0)" ::: "memory"); STAMP(7); BAR(); }
      }
    }
    STAMP(1);
    const int fr = lane & 15, fq = lane >> 4;
    const int f = (fr >> 1) & 7;
    const int xa0 = LDS_X + wid * 64 * 128 + (fr >> 3) * 1024 + (fr & 7) * 128 + ((fq ^ f) * 16);
    const int xa1 = xa0 ^ 64;
    const int wae = LDS_W + fq * 256 + (fr ^ (2 * (fr >> 3))) * 16;
    const int wao = LDS_W + fq * 256 + (fr ^ (1 + 2 * (fr >> 3))) * 16;
    f32x4 acc[4][7];
#pragma unroll
    for (int m = 0; m < 4; ++m)
#pragma unroll
      for (int t = 0; t < 7; ++t) { acc[m][t] = f32x4{0.f, 0.f, 0.f, 0.f}; asm volatile("" : "+v"(acc[m][t])); }
    bf16x8 xf0[4], wf0[7], xf1[4], wf1[7];
#define RD(p) (*(const LAS bf16x8*)(lds + (p)))
#define LOADF(xf, wf, xa, wofs) do { _Pragma("unroll") for (int t = 0; t < 7; ++t) wf[t] = RD(((t & 1) ? wao : wae) + (wofs) + t * 1024); \
                                     _Pragma("unroll") for (int m = 0; m < 4; ++m) xf[m] = RD((xa) + m * 2048); } while (0)
#define MMA(xf, wf) do { _Pragma("unroll") for (int m = 0; m < 4; ++m) _Pragma("unroll") for (int t = 0; t < 7; ++t) \
      acc[m][t] = __builtin_amdgcn_mfma_f32_16x16x32_bf16(wf[t], xf[m], acc[m][t], 0, 0, 0); } while (0)
#define INTERLEAVE() do { _Pragma("unroll") for (int i = 0; i < 11; ++i) { __builtin_amdgcn_sched_group_barrier(0x8, 1, 0); __builtin_amdgcn_sched_group_barrier(0x100, 1, 0); } \
      __builtin_amdgcn_sched_group_barrier(0x8, 17, 0); } while (0)
    LBAR();
    STAMP(2);
    LOADF(xf0, wf0, xa0, 0);
    int xo = 0, wo = 0;
#pragma unroll 1
    for (int s = 0; s < NS - 1; ++s) {
      LOADF(xf1, wf1, xa1 + xo, wo + 7168);
      MMA(xf0, wf0);
      INTERLEAVE();
      xo = (xo == 2 * XSLOT) ? 0 : xo + XSLOT;
      wo = wo ^ WSLOT;
      SCHED();
      asm volatile("s_waitcnt lgkmcnt(0)" ::: "memory");
      LBAR();
      SCHED();
      LOADF(xf0, wf0, xa0 + xo, wo);
      MMA(xf1, wf1);
      INTERLEAVE();
      SCHED();
    }
    LOADF(xf1, wf1, xa1 + xo, wo + 7168);
    MMA(xf0, wf0);
    INTERLEAVE();
    MMA(xf1, wf1);
    {
      const int sb = wid < 3 ? LDS_X + 2 * XSLOT + wid * (64 * 240) : LDS_T;
      const int wa_ = sb + fr * 240 + fq * 16, wb_ = sb + fr * 240 + 192 + fq * 8;
      const int r4 = lane / 14, c14 = lane % 14;
      const int ra = sb + r4 * 240 + c14 * 16;
      const unsigned vo = (unsigned)(((mb * BM + wid * 64 + r4) * N_ + jb * BN) * 2 + c14 * 16) | ((unsigned)(lane >= 56) << 31);
      const __amdgpu_buffer_rsrc_t rs = __builtin_amdgcn_make_buffer_rsrc((void*)out, 0, M_ * N_ * 2, 0x00020000);
#pragma unroll
      for (int m = 0; m < 4; ++m) {
#pragma unroll
        for (int pp = 0; pp < 3; ++pp) {
          u32x4 o;
          o.x = cvt_pk_bf16(acc[m][2 * pp][0], acc[m][2 * pp][1]); o.y = cvt_pk_bf16(acc[m][2 * pp][2], acc[m][2 * pp][3]);
          o.z = cvt_pk_bf16(acc[m][2 * pp + 1][0], acc[m][2 * pp + 1][1]); o.w = cvt_pk_bf16(acc[m][2 * pp + 1][2], acc[m][2 * pp + 1][3]);
          *(LAS u32x4*)(lds + wa_ + m * (16 * 240) + pp * 64) = o;
        }
        u32x2 o2;
        o2.x = cvt_pk_bf16(acc[m][6][0], acc[m][6][1]); o2.y = cvt_pk_bf16(acc[m][6][2], acc[m][6][3]);
        *(LAS u32x2*)(lds + wb_ + m * (16 * 240)) = o2;
        u32x4 v[4];
#pragma unroll
        for (int i = 0; i < 4; ++i) v[i] = *(const LAS u32x4*)(lds + ra + (m * 4 + i) * (4 * 240));
#pragma unroll
        for (int i = 0; i < 4; ++i) __builtin_amdgcn_raw_buffer_store_b128(v[i], rs, vo, (m * 4 + i) * 4 * N_ * 2, STPOL);
      }
    }
  }
}

extern "C" void kernel_launch(void* const* d_in, const int* in_sizes, int n_in, void* d_out, int out_size, void* d_ws,
                              size_t ws_size, hipStream_t stream) {
  const unsigned short* x = (const unsigned short*)d_in[0];
  const int* qw = (const int*)d_in[1];
  const unsigned short* cb = (const unsigned short*)d_in[2];
  const unsigned short* absmax = (const unsigned short*)d_in[3];
  unsigned short* out = (unsigned short*)d_out;
  (void)hipFuncSetAttribute((const void*)k_fused, hipFuncAttributeMaxDynamicSharedMemorySize, LDS_BYTES);
  k_fused<<<256, 512, LDS_BYTES, stream>>>(x, qw, cb, absmax, out
  );
}
```

```cpp
#define XMAP 0
#include <hip/hip_runtime.h>
#include <hip/hip_bf16.h>
#include <stdint.h>

typedef short bf16x8 __attribute__((ext_vector_type(8)));
typedef float f32x4 __attribute__((ext_vector_type(4)));
typedef float f32x2 __attribute__((ext_vector_type(2)));
typedef __bf16 bf16x2_t __attribute__((ext_vector_type(2)));
typedef int i32x4 __attribute__((ext_vector_type(4)));
typedef unsigned u32x4 __attribute__((ext_vector_type(4)));
typedef unsigned u32x2 __attribute__((ext_vector_type(2)));
#define LAS __attribute__((address_space(3)))

#define ORDER 1
#define EPI 1
#define STPOL 0
constexpr int M_ = 2048, K_ = 1024, N_ = 3584;
constexpr int BM = 256, BN = 112, NS = K_ / 64;
constexpr int XSLOT = 32768, WSLOT = 14336, TAB_G = 3584;
constexpr int LDS_X = 0, LDS_W = 3 * XSLOT, LDS_T = LDS_W + 2 * WSLOT;
constexpr int LDS_D = LDS_T + 8 * TAB_G;
constexpr int LDS_BYTES = LDS_D + 1024;

__device__ __forceinline__ unsigned cvt_pk_bf16(float lo, float hi) {
  const bf16x2_t v = __builtin_convertvector(f32x2{lo, hi}, bf16x2_t);
  return __builtin_bit_cast(unsigned, v);
}
__device__ __forceinline__ i32x4 make_srd(const void* p, unsigned bytes) {
  unsigned long long a = (unsigned long long)p;
  i32x4 r;
  r.x = __builtin_amdgcn_readfirstlane((int)(unsigned)a);
  r.y = __builtin_amdgcn_readfirstlane((int)(unsigned)(a >> 32));
  r.z = __builtin_amdgcn_readfirstlane((int)bytes);
  r.w = 0x00020000;
  return r;
}
__device__ __forceinline__ unsigned perm(unsigned s0, unsigned s1, unsigned sel) { return __builtin_amdgcn_perm(s0, s1, sel); }

#define STAMP(i)

#define XPOL ""
#define QPOL ""
#define DMA16(voff, srd, soff, imm) asm volatile("buffer_load_dwordx4 %0, %1, %2 offen offset:" #imm " " XPOL " lds" ::"v"(voff), "s"(srd), "s"(soff) : "memory")
#define QLOAD(dst, voff, srd, soff) asm volatile("buffer_load_dwordx4 %0, %1, %2, %3 offen " QPOL : "=v"(dst) : "v"(voff), "s"(srd), "s"(soff))
#define SETM0(val) asm volatile("s_mov_b32 m0, %0\n\ts_nop 0" ::"s"(val) : "memory")
#define XREAL(c) (c)
#define QREAL(c) (c)
#define WAITV(n) asm volatile("s_waitcnt vmcnt(" #n ")" ::: "memory")
#define WAITRAW(n, R) WAITRAW_(n, R)
#define WAITRAW_(n, R) asm volatile("s_waitcnt vmcnt(" #n ")" : "+v"(R[0]), "+v"(R[1]), "+v"(R[2]), "+v"(R[3]), "+v"(R[4]), "+v"(R[5]), "+v"(R[6]), "+v"(R[7]))
#define BAR() do { asm volatile("" ::: "memory"); __builtin_amdgcn_s_barrier(); asm volatile("" ::: "memory"); } while (0)
#define LBAR() BAR()
#define SCHED() __builtin_amdgcn_sched_barrier(0)

__global__ __launch_bounds__(512, 2) void k_fused(const unsigned short* __restrict__ x, const int* __restrict__ qw,
                                                  const unsigned short* __restrict__ cb, const unsigned short* __restrict__ absmax,
                                                  unsigned short* __restrict__ out
) {
  extern __shared__ __attribute__((aligned(16))) char smem[];
  LAS char* lds = (LAS char*)smem;
  const int bid = blockIdx.x, xcd = bid & 7, idx = bid >> 3;
  const int mb = idx & 7, jb = xcd * 4 + (idx >> 3);
  const int tid = threadIdx.x, lane = tid & 63;
  const int wid = __builtin_amdgcn_readfirstlane(tid >> 6);
  STAMP(wid >= 4 ? 8 : 0);
  if (wid >= 4) {
    const int L = wid - 4;
    const i32x4 srdx = make_srd(x, (unsigned)(M_ * K_ * 2));
    const i32x4 srdq = make_srd(qw, (unsigned)(K_ * N_ * 4));
    i32x4 srdx0 = srdx, srdq0 = srdq; srdx0.z = 0; srdq0.z = 0;
    unsigned vx[4];
#pragma unroll
    for (int i = 0; i < 4; ++i) {
      const int row = L * 64 + i * 8 + (lane >> 3);
      const int c = (lane & 7) ^ ((row >> 1) & 7);
      vx[i] = (unsigned)((mb * BM + row) * (K_ * 2) + c * 16 - i * 1024);
    }
    const int c4 = lane & 31, kg = 2 * L + (lane >> 5);
    const bool act = c4 < 28;
    const unsigned vq = (unsigned)((kg * 8 * N_ + jb * BN + c4 * 4) * 4) | ((unsigned)(!act) << 31);
    int tile, a;
    if (c4 < 24) { tile = 2 * (c4 >> 3) + (c4 & 1); a = (c4 & 7) >> 1; } else { tile = 6; a = c4 & 3; }
    const int v2 = (tile & 1) + 2 * (a >> 1);
    const int wdst = LDS_W + (kg >> 2) * 7168 + tile * 1024 + (kg & 3) * 256 + a * 64;
    int wd[2][4];
#pragma unroll
    for (int i = 0; i < 4; ++i) { wd[0][i] = act ? wdst + ((i ^ v2) * 16) : LDS_D + lane * 16; wd[1][i] = act ? wdst + WSLOT + ((i ^ v2) * 16) : LDS_D + lane * 16; }
    const int tadr = LDS_T + c4 * 16;
    const unsigned long long qmask = 0x0fffffff0fffffffull;

    unsigned vqr[8];
#pragma unroll
    for (int r = 0; r < 8; ++r) vqr[r] = vq + r * (N_ * 4);
    i32x4 rawA[8], rawB[8];
#define XPIECE(j, srd, sx_, sx2_) do { if (((j) & 3) == 0) DMA16(vx[0], srd, ((j) < 4 ? sx_ : sx2_), 0); if (((j) & 3) == 1) DMA16(vx[1], srd, ((j) < 4 ? sx_ : sx2_), 1024); \
                   if (((j) & 3) == 2) DMA16(vx[2], srd, ((j) < 4 ? sx_ : sx2_), 2048); if (((j) & 3) == 3) DMA16(vx[3], srd, ((j) < 4 ? sx_ : sx2_), 3072); } while (0)
    {
      const unsigned mx = (unsigned)(LDS_X + L * 8192), mx2 = mx + 4096, z = 0, z2 = 32 * K_ * 2;
      SETM0(mx); XPIECE(0, srdx, z, z2); XPIECE(1, srdx, z, z2); XPIECE(2, srdx, z, z2); XPIECE(3, srdx, z, z2);
      SETM0(mx2); XPIECE(4, srdx, z, z2); XPIECE(5, srdx, z, z2); XPIECE(6, srdx, z, z2); XPIECE(7, srdx, z, z2);
#pragma unroll
      for (int r = 0; r < 8; ++r) QLOAD(rawA[r], vqr[r], srdq, z);
      const unsigned s1 = 64 * N_ * 4;
#pragma unroll
      for (int r = 0; r < 8; ++r) QLOAD(rawB[r], vqr[r], srdq, s1);
    }
    BAR();
    WAITV(8);
    STAMP(9);
    unsigned xm = (unsigned)(LDS_X + XSLOT + L * 8192);
    unsigned sx = 128, sq = 2 * 64 * N_ * 4;
    i32x4 srdX = srdx, srdQ = srdq;
#define TS(i)
#define PK(i) do { pk[i][0] = perm(R_[1][i], R_[0][i], 0x0c0c0400u) | perm(R_[3][i], R_[2][i], 0x04000c0cu); \
                   pk[i][1] = perm(R_[5][i], R_[4][i], 0x0c0c0400u) | perm(R_[7][i], R_[6][i], 0x04000c0cu); } while (0)
#define LA(i, h) do { c1 = pk[i][h] ^ 0x08080808u; lo = perm(tl[i][1], tl[i][0], pk[i][h]) ^ perm(tl[i][3], tl[i][2], c1); } while (0)
#define LB(i, h) do { const unsigned hi = perm(th[i][1], th[i][0], pk[i][h]) ^ perm(th[i][3], th[i][2], c1); \
                      o[2 * h] = perm(hi, lo, 0x05010400u); o[2 * h + 1] = perm(hi, lo, 0x07030602u); } while (0)
#define WR(i) do { *(LAS u32x4*)(lds + wd[wsel_][i]) = o; } while (0)
#define XD(j) do { SCHED(); if ((j) == 4) SETM0(xm + 4096); XPIECE(j, srdX, sx, sx + 32 * K_ * 2); SCHED(); } while (0)
#define QD(r) do { SCHED(); QLOAD(R_[r], vqr[r], srdQ, sq); SCHED(); } while (0)
#define TAILSC1 SCHED(); const unsigned sxn = sx + 128, sqn = sq + 64 * N_ * 4; \
      const unsigned xmn = (xm >= (unsigned)(LDS_X + 2 * XSLOT)) ? xm - 2 * XSLOT : xm + XSLOT;
#define WR_N 18
#define WAIT_X() WAITV(19)
#define ORDER_BODY \
      PK(0); PK(1); XD(2); PK(2); PK(3); QD(0); TS(2); \
      LA(0, 0); LB(0, 0); XD(3); LA(0, 1); QD(1); LB(0, 1); WR(0); \
      XD(4); LA(1, 0); LB(1, 0); QD(2); LA(1, 1); XD(5); LB(1, 1); WR(1); \
      QD(3); LA(2, 0); LB(2, 0); XD(6); LA(2, 1); QD(4); LB(2, 1); WR(2); \
      XD(7); LA(3, 0); LB(3, 0); QD(5); LA(3, 1); LB(3, 1); WR(3); \
      TAILSC1 QD(6); QD(7); \
      xm = xmn; srdX.z = XREAL(sxn < NS * 128) ? srdx.z : 0; srdQ.z = QREAL(sqn < (unsigned)NS * 64 * N_ * 4) ? srdq.z : 0; sx = sxn; sq = sqn;
#define ITER(R, wsel) do { \
      i32x4 (&R_)[8] = R; constexpr int wsel_ = wsel; \
      unsigned pk[4][2], c1, lo; u32x4 o; \
      XD(0); XD(1); \
      if (wsel == 0) { asm volatile("" : "+v"(tcur)); \
        _Pragma("unroll") for (int i = 0; i < 4; ++i) { tl[i] = *(const LAS u32x4*)(lds + tcur + (2 * i) * 448); th[i] = *(const LAS u32x4*)(lds + tcur + (2 * i + 1) * 448); } \
        tcur += TAB_G; SCHED(); } \
      TS(0); WAITRAW(WR_N, R_); TS(1); \
      ORDER_BODY \
      SETM0(xm); \
      TS(3); WAIT_X(); TS(4); \
      asm volatile("s_waitcnt lgkmcnt(0)" ::: "memory"); TS(5); \
      LBAR(); TS(6); } while (0)
    SETM0(xm);
    int tcur = tadr;
#pragma unroll 1
    for (int g = 0; g < 8; ++g) {
      u32x4 tl[4], th[4];
      ITER(rawA, 0);
      ITER(rawB, 1);
    }
    WAITV(0);
    STAMP(10);
  } else {
    {
      const u32x4 cbr0 = *(const u32x4*)cb, cbr1 = *(const u32x4*)(cb + 8);
      float cf[16];
#pragma unroll
      for (int j = 0; j < 4; ++j) { cf[2 * j] = __uint_as_float(cbr0[j] << 16); cf[2 * j + 1] = __uint_as_float(cbr0[j] & 0xffff0000u);
                                    cf[8 + 2 * j] = __uint_as_float(cbr1[j] << 16); cf[8 + 2 * j + 1] = __uint_as_float(cbr1[j] & 0xffff0000u); }
      unsigned short am[4];
#pragma unroll
      for (int i = 0; i < 4; ++i) { const int p = tid + 256 * i; const int pp = p < 896 ? p : 0; am[i] = absmax[(pp / 112) * N_ + jb * BN + pp % 112]; }
      auto plane = [](u32x4 t) {
        const unsigned a1 = t[1] ^ 0xFFFFFF00u, b1 = t[3] ^ 0xFFFFFF00u;
        unsigned a0 = t[0], b0 = t[2];
#pragma unroll
        for (int it = 0; it < 3; ++it) { const unsigned na = t[0] ^ perm(b1, b0, 0x0B0A0908u), nb = t[2] ^ perm(a1, a0, 0x0B0A0908u); a0 = na; b0 = nb; }
        return u32x4{a0, a1, b0, b1};
      };
#pragma unroll
      for (int i = 0; i < 4; ++i) {
        const int p = tid + 256 * i;
        const float s = __uint_as_float(((unsigned)am[i]) << 16);
        unsigned d[8];
#pragma unroll
        for (int j = 0; j < 8; ++j) d[j] = cvt_pk_bf16(cf[2 * j] * s, cf[2 * j + 1] * s);
        u32x4 lo, hi;
#pragma unroll
        for (int j = 0; j < 4; ++j) { lo[j] = perm(d[2 * j + 1], d[2 * j], 0x06040200u); hi[j] = perm(d[2 * j + 1], d[2 * j], 0x07050301u); }
        lo = plane(lo); hi = plane(hi);
        if (p < 896) {
          const int g = p / 112, n = p % 112;
          const int ta = LDS_T + g * TAB_G + (n & 3) * 2 * 448 + (n >> 2) * 16;
          *(LAS u32x4*)(lds + ta) = lo;
          *(LAS u32x4*)(lds + ta + 448) = hi;
        }
        if (i == 0) { asm volatile("s_waitcnt lgkmcnt(0)" ::: "memory"); STAMP(7); BAR(); }
      }
    }
    STAMP(1);
    const int fr = lane & 15, fq = lane >> 4;
    const int f = (fr >> 1) & 7;
    const int xa0 = LDS_X + wid * 64 * 128 + (fr >> 3) * 1024 + (fr & 7) * 128 + ((fq ^ f) * 16);
    const int xa1 = xa0 ^ 64;
    const int wae = LDS_W + fq * 256 + (fr ^ (2 * (fr >> 3))) * 16;
    const int wao = LDS_W + fq * 256 + (fr ^ (1 + 2 * (fr >> 3))) * 16;
    f32x4 acc[4][7];
#pragma unroll
    for (int m = 0; m < 4; ++m)
#pragma unroll
      for (int t = 0; t < 7; ++t) { acc[m][t] = f32x4{0.f, 0.f, 0.f, 0.f}; asm volatile("" : "+v"(acc[m][t])); }
    bf16x8 xf0[4], wf0[7], xf1[4], wf1[7];
#define RD(p) (*(const LAS bf16x8*)(lds + (p)))
#define LOADF(xf, wf, xa, wofs) do { _Pragma("unroll") for (int t = 0; t < 7; ++t) wf[t] = RD(((t & 1) ? wao : wae) + (wofs) + t * 1024); \
                                     _Pragma("unroll") for (int m = 0; m < 4; ++m) xf[m] = RD((xa) + m * 2048); } while (0)
#define MMA(xf, wf) do { _Pragma("unroll") for (int m = 0; m < 4; ++m) _Pragma("unroll") for (int t = 0; t < 7; ++t) \
      acc[m][t] = __builtin_amdgcn_mfma_f32_16x16x32_bf16(wf[t], xf[m], acc[m][t], 0, 0, 0); } while (0)
#define INTERLEAVE() do { _Pragma("unroll") for (int i = 0; i < 11; ++i) { __builtin_amdgcn_sched_group_barrier(0x8, 1, 0); __builtin_amdgcn_sched_group_barrier(0x100, 1, 0); } \
      __builtin_amdgcn_sched_group_barrier(0x8, 17, 0); } while (0)
    LBAR();
    STAMP(2);
    LOADF(xf0, wf0, xa0, 0);
    int xo = 0, wo = 0;
#pragma unroll 1
    for (int s = 0; s < NS - 1; ++s) {
      LOADF(xf1, wf1, xa1 + xo, wo + 7168);
      MMA(xf0, wf0);
      INTERLEAVE();
      xo = (xo == 2 * XSLOT) ? 0 : xo + XSLOT;
      wo = wo ^ WSLOT;
      SCHED();
      asm volatile("s_waitcnt lgkmcnt(0)" ::: "memory");
      LBAR();
      SCHED();
      LOADF(xf0, wf0, xa0 + xo, wo);
      MMA(xf1, wf1);
      INTERLEAVE();
      SCHED();
    }
    LOADF(xf1, wf1, xa1 + xo, wo + 7168);
    MMA(xf0, wf0);
    INTERLEAVE();
    MMA(xf1, wf1);
    {
      const int sb = wid < 3 ? LDS_X + 2 * XSLOT + wid * (64 * 240) : LDS_T;
      const int wa_ = sb + fr * 240 + fq * 16, wb_ = sb + fr * 240 + 192 + fq * 8;
      const int r4 = lane / 14, c14 = lane % 14;
      const int ra = sb + r4 * 240 + c14 * 16;
      const unsigned vo = (unsigned)(((mb * BM + wid * 64 + r4) * N_ + jb * BN) * 2 + c14 * 16) | ((unsigned)(lane >= 56) << 31);
      const __amdgpu_buffer_rsrc_t rs = __builtin_amdgcn_make_buffer_rsrc((void*)out, 0, M_ * N_ * 2, 0x00020000);
#pragma unroll
      for (int m = 0; m < 4; ++m) {
#pragma unroll
        for (int pp = 0; pp < 3; ++pp) {
          u32x4 o;
          o.x = cvt_pk_bf16(acc[m][2 * pp][0], acc[m][2 * pp][1]); o.y = cvt_pk_bf16(acc[m][2 * pp][2], acc[m][2 * pp][3]);
          o.z = cvt_pk_bf16(acc[m][2 * pp + 1][0], acc[m][2 * pp + 1][1]); o.w = cvt_pk_bf16(acc[m][2 * pp + 1][2], acc[m][2 * pp + 1][3]);
          *(LAS u32x4*)(lds + wa_ + m * (16 * 240) + pp * 64) = o;
        }
        u32x2 o2;
        o2.x = cvt_pk_bf16(acc[m][6][0], acc[m][6][1]); o2.y = cvt_pk_bf16(acc[m][6][2], acc[m][6][3]);
        *(LAS u32x2*)(lds + wb_ + m * (16 * 240)) = o2;
        u32x4 v[4];
#pragma unroll
        for (int i = 0; i < 4; ++i) v[i] = *(const LAS u32x4*)(lds + ra + (m * 4 + i) * (4 * 240));
#pragma unroll
        for (int i = 0; i < 4; ++i) __builtin_amdgcn_raw_buffer_store_b128(v[i], rs, vo, (m * 4 + i) * 4 * N_ * 2, STPOL);
      }
    }
  }
}

extern "C" void kernel_launch(void* const* d_in, const int* in_sizes, int n_in, void* d_out, int out_size, void* d_ws,
                              size_t ws_size, hipStream_t stream) {
  const unsigned short* x = (const unsigned short*)d_in[0];
  const int* qw = (const int*)d_in[1];
  const unsigned short* cb = (const unsigned short*)d_in[2];
  const unsigned short* absmax = (const unsigned short*)d_in[3];
  unsigned short* out = (unsigned short*)d_out;
  (void)hipFuncSetAttribute((const void*)k_fused, hipFuncAttributeMaxDynamicSharedMemorySize, LDS_BYTES);
  k_fused<<<256, 512, LDS_BYTES, stream>>>(x, qw, cb, absmax, out
  );
}
```

```cpp
#define TLBW 1
#include <hip/hip_runtime.h>
#include <hip/hip_bf16.h>
#include <stdint.h>

typedef short bf16x8 __attribute__((ext_vector_type(8)));
typedef float f32x4 __attribute__((ext_vector_type(4)));
typedef float f32x2 __attribute__((ext_vector_type(2)));
typedef __bf16 bf16x2_t __attribute__((ext_vector_type(2)));
typedef int i32x4 __attribute__((ext_vector_type(4)));
typedef unsigned u32x4 __attribute__((ext_vector_type(4)));
typedef unsigned u32x2 __attribute__((ext_vector_type(2)));
#define LAS __attribute__((address_space(3)))

#define XMAP 1
#define ORDER 1
#define EPI 1
#define STPOL 0
constexpr int M_ = 2048, K_ = 1024, N_ = 3584;
constexpr int BM = 256, BN = 112, NS = K_ / 64;
constexpr int XSLOT = 32768, WSLOT = 14336, TAB_G = 3584;
constexpr int LDS_X = 0, LDS_W = 3 * XSLOT, LDS_T = LDS_W + 2 * WSLOT;
constexpr int LDS_D = LDS_T + 8 * TAB_G;
constexpr int LDS_BYTES = LDS_D + 1024;

__device__ __forceinline__ unsigned cvt_pk_bf16(float lo, float hi) {
  const bf16x2_t v = __builtin_convertvector(f32x2{lo, hi}, bf16x2_t);
  return __builtin_bit_cast(unsigned, v);
}
__device__ __forceinline__ i32x4 make_srd(const void* p, unsigned bytes) {
  unsigned long long a = (unsigned long long)p;
  i32x4 r;
  r.x = __builtin_amdgcn_readfirstlane((int)(unsigned)a);
  r.y = __builtin_amdgcn_readfirstlane((int)(unsigned)(a >> 32));
  r.z = __builtin_amdgcn_readfirstlane((int)bytes);
  r.w = 0x00020000;
  return r;
}
__device__ __forceinline__ unsigned perm(unsigned s0, unsigned s1, unsigned sel) { return __builtin_amdgcn_perm(s0, s1, sel); }

#define STAMP(i)

#define XPOL ""
#define QPOL ""
#define DMA16(voff, srd, soff, imm) asm volatile("buffer_load_dwordx4 %0, %1, %2 offen offset:" #imm " " XPOL " lds" ::"v"(voff), "s"(srd), "s"(soff) : "memory")
#define QLOAD(dst, voff, srd, soff) asm volatile("buffer_load_dwordx4 %0, %1, %2, %3 offen " QPOL : "=v"(dst) : "v"(voff), "s"(srd), "s"(soff))
#define SETM0(val) asm volatile("s_mov_b32 m0, %0\n\ts_nop 0" ::"s"(val) : "memory")
#define XREAL(c) (c)
#define QREAL(c) (c)
#define WAITV(n) asm volatile("s_waitcnt vmcnt(" #n ")" ::: "memory")
#define WAITRAW(n, R) WAITRAW_(n, R)
#define WAITRAW_(n, R) asm volatile("s_waitcnt vmcnt(" #n ")" : "+v"(R[0]), "+v"(R[1]), "+v"(R[2]), "+v"(R[3]), "+v"(R[4]), "+v"(R[5]), "+v"(R[6]), "+v"(R[7]))
#define BAR() do { asm volatile("" ::: "memory"); __builtin_amdgcn_s_barrier(); asm volatile("" ::: "memory"); } while (0)
#define LBAR() BAR()
#define SCHED() __builtin_amdgcn_sched_barrier(0)

__global__ __launch_bounds__(512, 2) void k_fused(const unsigned short* __restrict__ x, const int* __restrict__ qw,
                                                  const unsigned short* __restrict__ cb, const unsigned short* __restrict__ absmax,
                                                  unsigned short* __restrict__ out
) {
  extern __shared__ __attribute__((aligned(16))) char smem[];
  LAS char* lds = (LAS char*)smem;
  const int bid = blockIdx.x, xcd = bid & 7, idx = bid >> 3;
  const int mb = (xcd & 1) * 4 + (idx & 3), jb = (xcd >> 1) * 8 + (idx >> 2);
  const int tid = threadIdx.x, lane = tid & 63;
  const int wid = __builtin_amdgcn_readfirstlane(tid >> 6);
  STAMP(wid >= 4 ? 8 : 0);
  if (wid >= 4) {
    const int L = wid - 4;
    const i32x4 srdx = make_srd(x, (unsigned)(M_ * K_ * 2));
    const i32x4 srdq = make_srd(qw, (unsigned)(K_ * N_ * 4));
    i32x4 srdx0 = srdx, srdq0 = srdq; srdx0.z = 0; srdq0.z = 0;
    unsigned vx[4];
#pragma unroll
    for (int i = 0; i < 4; ++i) {
      const int row = L * 64 + i * 8 + (lane >> 3);
      const int c = (lane & 7) ^ ((row >> 1) & 7);
      vx[i] = (unsigned)((mb * BM + row) * (K_ * 2) + c * 16 - i * 1024);
    }
    const int c4 = lane & 31, kg = 2 * L + (lane >> 5);
    const bool act = c4 < 28;
    const unsigned vq = (unsigned)((kg * 8 * N_ + jb * BN + c4 * 4) * 4) | ((unsigned)(!act) << 31);
    int tile, a;
    if (c4 < 24) { tile = 2 * (c4 >> 3) + (c4 & 1); a = (c4 & 7) >> 1; } else { tile = 6; a = c4 & 3; }
    const int v2 = (tile & 1) + 2 * (a >> 1);
    const int wdst = LDS_W + (kg >> 2) * 7168 + tile * 1024 + (kg & 3) * 256 + a * 64;
    int wd[2][4];
#pragma unroll
    for (int i = 0; i < 4; ++i) { wd[0][i] = act ? wdst + ((i ^ v2) * 16) : LDS_D + lane * 16; wd[1][i] = act ? wdst + WSLOT + ((i ^ v2) * 16) : LDS_D + lane * 16; }
    const int tadr = LDS_T + c4 * 16;
    const unsigned long long qmask = 0x0fffffff0fffffffull;

    unsigned vqr[8];
#pragma unroll
    for (int r = 0; r < 8; ++r) vqr[r] = vq + r * (N_ * 4);
    i32x4 rawA[8], rawB[8];
#define XPIECE(j, srd, sx_, sx2_) do { if (((j) & 3) == 0) DMA16(vx[0], srd, ((j) < 4 ? sx_ : sx2_), 0); if (((j) & 3) == 1) DMA16(vx[1], srd, ((j) < 4 ? sx_ : sx2_), 1024); \
                   if (((j) & 3) == 2) DMA16(vx[2], srd, ((j) < 4 ? sx_ : sx2_), 2048); if (((j) & 3) == 3) DMA16(vx[3], srd, ((j) < 4 ? sx_ : sx2_), 3072); } while (0)
    {
      const unsigned mx = (unsigned)(LDS_X + L * 8192), mx2 = mx + 4096, z = 0, z2 = 32 * K_ * 2;
      SETM0(mx); XPIECE(0, srdx, z, z2); XPIECE(1, srdx, z, z2); XPIECE(2, srdx, z, z2); XPIECE(3, srdx, z, z2);
      SETM0(mx2); XPIECE(4, srdx, z, z2); XPIECE(5, srdx, z, z2); XPIECE(6, srdx, z, z2); XPIECE(7, srdx, z, z2);
#pragma unroll
      for (int r = 0; r < 8; ++r) QLOAD(rawA[r], vqr[r], srdq, z);
      const unsigned s1 = 64 * N_ * 4;
#pragma unroll
      for (int r = 0; r < 8; ++r) QLOAD(rawB[r], vqr[r], srdq, s1);
    }
    BAR();
    WAITV(8);
    STAMP(9);
    unsigned xm = (unsigned)(LDS_X + XSLOT + L * 8192);
    unsigned sx = 128, sq = 2 * 64 * N_ * 4;
    i32x4 srdX = srdx, srdQ = srdq;
#define TS(i)
#define PK(i) do { pk[i][0] = perm(R_[1][i], R_[0][i], 0x0c0c0400u) | perm(R_[3][i], R_[2][i], 0x04000c0cu); \
                   pk[i][1] = perm(R_[5][i], R_[4][i], 0x0c0c0400u) | perm(R_[7][i], R_[6][i], 0x04000c0cu); } while (0)
#define LA(i, h) do { c1 = pk[i][h] ^ 0x08080808u; lo = perm(tl[i][1], tl[i][0], pk[i][h]) ^ perm(tl[i][3], tl[i][2], c1); } while (0)
#define LB(i, h) do { const unsigned hi = perm(th[i][1], th[i][0], pk[i][h]) ^ perm(th[i][3], th[i][2], c1); \
                      o[2 * h] = perm(hi, lo, 0x05010400u); o[2 * h + 1] = perm(hi, lo, 0x07030602u); } while (0)
#define WR(i) do { *(LAS u32x4*)(lds + wd[wsel_][i]) = o; } while (0)
#define XD(j) do { SCHED(); if ((j) == 4) SETM0(xm + 4096); XPIECE(j, srdX, sx, sx + 32 * K_ * 2); SCHED(); } while (0)
#define QD(r) do { SCHED(); QLOAD(R_[r], vqr[r], srdQ, sq); SCHED(); } while (0)
#define TAILSC1 SCHED(); const unsigned sxn = sx + 128, sqn = sq + 64 * N_ * 4; \
      const unsigned xmn = (xm >= (unsigned)(LDS_X + 2 * XSLOT)) ? xm - 2 * XSLOT : xm + XSLOT;
#define WR_N 18
#define WAIT_X() WAITV(19)
#define ORDER_BODY \
      PK(0); PK(1); XD(2); PK(2); PK(3); QD(0); TS(2); \
      LA(0, 0); LB(0, 0); XD(3); LA(0, 1); QD(1); LB(0, 1); WR(0); \
      XD(4); LA(1, 0); LB(1, 0); QD(2); LA(1, 1); XD(5); LB(1, 1); WR(1); \
      QD(3); LA(2, 0); LB(2, 0); XD(6); LA(2, 1); QD(4); LB(2, 1); WR(2); \
      XD(7); LA(3, 0); LB(3, 0); QD(5); LA(3, 1); LB(3, 1); WR(3); \
      TAILSC1 QD(6); QD(7); \
      xm = xmn; srdX.z = XREAL(sxn < NS * 128) ? srdx.z : 0; srdQ.z = QREAL(sqn < (unsigned)NS * 64 * N_ * 4) ? srdq.z : 0; sx = sxn; sq = sqn;
#define ITER(R, wsel) do { \
      i32x4 (&R_)[8] = R; constexpr int wsel_ = wsel; \
      unsigned pk[4][2], c1, lo; u32x4 o; \
      XD(0); XD(1); \
      if (wsel == 0) { asm volatile("" : "+v"(tcur)); \
        _Pragma("unroll") for (int i = 0; i < 4; ++i) { tl[i] = *(const LAS u32x4*)(lds + tcur + (2 * i) * 448); th[i] = *(const LAS u32x4*)(lds + tcur + (2 * i + 1) * 448); } \
        tcur += TAB_G; SCHED(); } \
      TS(0); WAITRAW(WR_N, R_); TS(1); \
      ORDER_BODY \
      SETM0(xm); \
      TS(3); WAIT_X(); TS(4); \
      asm volatile("s_waitcnt lgkmcnt(0)" ::: "memory"); TS(5); \
      LBAR(); TS(6); } while (0)
    SETM0(xm);
    int tcur = tadr;
#pragma unroll 1
    for (int g = 0; g < 8; ++g) {
      u32x4 tl[4], th[4];
      ITER(rawA, 0);
      ITER(rawB, 1);
    }
    WAITV(0);
    STAMP(10);
  } else {
    unsigned tlbw = 0;
    if (wid == 3) {
      const char* base; unsigned off;
      if (lane < 8)       { base = (const char*)qw;  off = (unsigned)(idx * 8 + lane) * 65536u;        if (off >= (unsigned)K_ * N_ * 4) off = 0; }
      else if (lane < 16) { base = (const char*)out; off = (unsigned)(idx * 8 + lane - 8) * 65536u;    if (off >= (unsigned)M_ * N_ * 2) off = 0; }
      else                { base = (const char*)x;   off = (unsigned)(idx * 2 + (lane & 1)) * 65536u; }
      unsigned t_;
      const char* a_ = base + off;
      asm volatile("global_load_dword %0, %1, off" : "=v"(t_) : "v"(a_));
      tlbw = t_;
    }
    {
      const u32x4 cbr0 = *(const u32x4*)cb, cbr1 = *(const u32x4*)(cb + 8);
      float cf[16];
#pragma unroll
      for (int j = 0; j < 4; ++j) { cf[2 * j] = __uint_as_float(cbr0[j] << 16); cf[2 * j + 1] = __uint_as_float(cbr0[j] & 0xffff0000u);
                                    cf[8 + 2 * j] = __uint_as_float(cbr1[j] << 16); cf[8 + 2 * j + 1] = __uint_as_float(cbr1[j] & 0xffff0000u); }
      unsigned short am[4];
#pragma unroll
      for (int i = 0; i < 4; ++i) { const int p = tid + 256 * i; const int pp = p < 896 ? p : 0; am[i] = absmax[(pp / 112) * N_ + jb * BN + pp % 112]; }
      auto plane = [](u32x4 t) {
        const unsigned a1 = t[1] ^ 0xFFFFFF00u, b1 = t[3] ^ 0xFFFFFF00u;
        unsigned a0 = t[0], b0 = t[2];
#pragma unroll
        for (int it = 0; it < 3; ++it) { const unsigned na = t[0] ^ perm(b1, b0, 0x0B0A0908u), nb = t[2] ^ perm(a1, a0, 0x0B0A0908u); a0 = na; b0 = nb; }
        return u32x4{a0, a1, b0, b1};
      };
#pragma unroll
      for (int i = 0; i < 4; ++i) {
        const int p = tid + 256 * i;
        const float s = __uint_as_float(((unsigned)am[i]) << 16);
        unsigned d[8];
#pragma unroll
        for (int j = 0; j < 8; ++j) d[j] = cvt_pk_bf16(cf[2 * j] * s, cf[2 * j + 1] * s);
        u32x4 lo, hi;
#pragma unroll
        for (int j = 0; j < 4; ++j) { lo[j] = perm(d[2 * j + 1], d[2 * j], 0x06040200u); hi[j] = perm(d[2 * j + 1], d[2 * j], 0x07050301u); }
        lo = plane(lo); hi = plane(hi);
        if (p < 896) {
          const int g = p / 112, n = p % 112;
          const int ta = LDS_T + g * TAB_G + (n & 3) * 2 * 448 + (n >> 2) * 16;
          *(LAS u32x4*)(lds + ta) = lo;
          *(LAS u32x4*)(lds + ta + 448) = hi;
        }
        if (i == 0) { asm volatile("s_waitcnt lgkmcnt(0)" ::: "memory"); STAMP(7); BAR(); }
      }
    }
    STAMP(1);
    const int fr = lane & 15, fq = lane >> 4;
    const int f = (fr >> 1) & 7;
    const int xa0 = LDS_X + wid * 64 * 128 + (fr >> 3) * 1024 + (fr & 7) * 128 + ((fq ^ f) * 16);
    const int xa1 = xa0 ^ 64;
    const int wae = LDS_W + fq * 256 + (fr ^ (2 * (fr >> 3))) * 16;
    const int wao = LDS_W + fq * 256 + (fr ^ (1 + 2 * (fr >> 3))) * 16;
    f32x4 acc[4][7];
#pragma unroll
    for (int m = 0; m < 4; ++m)
#pragma unroll
      for (int t = 0; t < 7; ++t) { acc[m][t] = f32x4{0.f, 0.f, 0.f, 0.f}; asm volatile("" : "+v"(acc[m][t])); }
    bf16x8 xf0[4], wf0[7], xf1[4], wf1[7];
#define RD(p) (*(const LAS bf16x8*)(lds + (p)))
#define LOADF(xf, wf, xa, wofs) do { _Pragma("unroll") for (int t = 0; t < 7; ++t) wf[t] = RD(((t & 1) ? wao : wae) + (wofs) + t * 1024); \
                                     _Pragma("unroll") for (int m = 0; m < 4; ++m) xf[m] = RD((xa) + m * 2048); } while (0)
#define MMA(xf, wf) do { _Pragma("unroll") for (int m = 0; m < 4; ++m) _Pragma("unroll") for (int t = 0; t < 7; ++t) \
      acc[m][t] = __builtin_amdgcn_mfma_f32_16x16x32_bf16(wf[t], xf[m], acc[m][t], 0, 0, 0); } while (0)
#define INTERLEAVE() do { _Pragma("unroll") for (int i = 0; i < 11; ++i) { __builtin_amdgcn_sched_group_barrier(0x8, 1, 0); __builtin_amdgcn_sched_group_barrier(0x100, 1, 0); } \
      __builtin_amdgcn_sched_group_barrier(0x8, 17, 0); } while (0)
    asm volatile("s_waitcnt vmcnt(0)" : "+v"(tlbw) :: "memory");
    LBAR();
    STAMP(2);
    LOADF(xf0, wf0, xa0, 0);
    int xo = 0, wo = 0;
#pragma unroll 1
    for (int s = 0; s < NS - 1; ++s) {
      LOADF(xf1, wf1, xa1 + xo, wo + 7168);
      MMA(xf0, wf0);
      INTERLEAVE();
      xo = (xo == 2 * XSLOT) ? 0 : xo + XSLOT;
      wo = wo ^ WSLOT;
      SCHED();
      asm volatile("s_waitcnt lgkmcnt(0)" ::: "memory");
      LBAR();
      SCHED();
      LOADF(xf0, wf0, xa0 + xo, wo);
      MMA(xf1, wf1);
      INTERLEAVE();
      SCHED();
    }
    LOADF(xf1, wf1, xa1 + xo, wo + 7168);
    MMA(xf0, wf0);
    INTERLEAVE();
    MMA(xf1, wf1);
    {
      const int sb = wid < 3 ? LDS_X + 2 * XSLOT + wid * (64 * 240) : LDS_T;
      const int wa_ = sb + fr * 240 + fq * 16, wb_ = sb + fr * 240 + 192 + fq * 8;
      const int r4 = lane / 14, c14 = lane % 14;
      const int ra = sb + r4 * 240 + c14 * 16;
      const unsigned vo = (unsigned)(((mb * BM + wid * 64 + r4) * N_ + jb * BN) * 2 + c14 * 16) | ((unsigned)(lane >= 56) << 31);
      const __amdgpu_buffer_rsrc_t rs = __builtin_amdgcn_make_buffer_rsrc((void*)out, 0, M_ * N_ * 2, 0x00020000);
#pragma unroll
      for (int m = 0; m < 4; ++m) {
#pragma unroll
        for (int pp = 0; pp < 3; ++pp) {
          u32x4 o;
          o.x = cvt_pk_bf16(acc[m][2 * pp][0], acc[m][2 * pp][1]); o.y = cvt_pk_bf16(acc[m][2 * pp][2], acc[m][2 * pp][3]);
          o.z = cvt_pk_bf16(acc[m][2 * pp + 1][0], acc[m][2 * pp + 1][1]); o.w = cvt_pk_bf16(acc[m][2 * pp + 1][2], acc[m][2 * pp + 1][3]);
          *(LAS u32x4*)(lds + wa_ + m * (16 * 240) + pp * 64) = o;
        }
        u32x2 o2;
        o2.x = cvt_pk_bf16(acc[m][6][0], acc[m][6][1]); o2.y = cvt_pk_bf16(acc[m][6][2], acc[m][6][3]);
        *(LAS u32x2*)(lds + wb_ + m * (16 * 240)) = o2;
        u32x4 v[4];
#pragma unroll
        for (int i = 0; i < 4; ++i) v[i] = *(const LAS u32x4*)(lds + ra + (m * 4 + i) * (4 * 240));
#pragma unroll
        for (int i = 0; i < 4; ++i) __builtin_amdgcn_raw_buffer_store_b128(v[i], rs, vo, (m * 4 + i) * 4 * N_ * 2, STPOL);
      }
    }
  }
}

extern "C" void kernel_launch(void* const* d_in, const int* in_sizes, int n_in, void* d_out, int out_size, void* d_ws,
                              size_t ws_size, hipStream_t stream) {
  const unsigned short* x = (const unsigned short*)d_in[0];
  const int* qw = (const int*)d_in[1];
  const unsigned short* cb = (const unsigned short*)d_in[2];
  const unsigned short* absmax = (const unsigned short*)d_in[3];
  unsigned short* out = (unsigned short*)d_out;
  (void)hipFuncSetAttribute((const void*)k_fused, hipFuncAttributeMaxDynamicSharedMemorySize, LDS_BYTES);
  k_fused<<<256, 512, LDS_BYTES, stream>>>(x, qw, cb, absmax, out
  );
}
```

```cpp
#include <hip/hip_runtime.h>
#include <hip/hip_bf16.h>
#include <stdint.h>
#include <type_traits>

typedef short bf16x8 __attribute__((ext_vector_type(8)));
typedef float f32x4 __attribute__((ext_vector_type(4)));
typedef float f32x2 __attribute__((ext_vector_type(2)));
typedef __bf16 bf16x2_t __attribute__((ext_vector_type(2)));
typedef int i32x4 __attribute__((ext_vector_type(4)));
typedef unsigned u32x4 __attribute__((ext_vector_type(4)));
#define LAS __attribute__((address_space(3)))

constexpr int M_ = 2048, K_ = 1024, N_ = 3584;
constexpr int BM = 256, BN = 128, NS = K_ / 64, NJB = N_ / BN;
constexpr int XSLOT = 32768, WSLOT = 16384, TAB_G = 4096;
constexpr int LDS_X = 0, LDS_W = 3 * XSLOT, LDS_T = LDS_W + 2 * WSLOT, LDS_BYTES = LDS_T + 8 * TAB_G;
static_assert(LDS_BYTES <= 163840, "LDS");
#define PFW 1
#define PFD 12
#define STPOL 16

__device__ __forceinline__ unsigned cvt_pk_bf16(float lo, float hi) {
  const bf16x2_t v = __builtin_convertvector(f32x2{lo, hi}, bf16x2_t);
  return __builtin_bit_cast(unsigned, v);
}
__device__ __forceinline__ i32x4 make_srd(const void* p, unsigned bytes) {
  unsigned long long a = (unsigned long long)p;
  i32x4 r;
  r.x = __builtin_amdgcn_readfirstlane((int)(unsigned)a);
  r.y = __builtin_amdgcn_readfirstlane((int)(unsigned)(a >> 32));
  r.z = __builtin_amdgcn_readfirstlane((int)bytes);
  r.w = 0x00020000;
  return r;
}
__device__ __forceinline__ unsigned perm(unsigned s0, unsigned s1, unsigned sel) { return __builtin_amdgcn_perm(s0, s1, sel); }

#define STAMP(i)

#define DMA16(voff, srd, soff, imm) asm volatile("buffer_load_dwordx4 %0, %1, %2 offen offset:" #imm " lds" ::"v"(voff), "s"(srd), "s"(soff) : "memory")
#define QLOAD(dst, voff, srd, soff) asm volatile("buffer_load_dwordx4 %0, %1, %2, %3 offen" : "=v"(dst) : "v"(voff), "s"(srd), "s"(soff))
#define SETM0(val) asm volatile("s_mov_b32 m0, %0" ::"s"(val) : "memory")
#define NOP0() asm volatile("s_nop 0")
#define BAR() do { asm volatile("" ::: "memory"); __builtin_amdgcn_s_barrier(); asm volatile("" ::: "memory"); } while (0)
#define SCHED() __builtin_amdgcn_sched_barrier(0)
#define IC(n) std::integral_constant<int, n>{}
template <int N> __device__ __forceinline__ void waitv() { static_assert(N >= 0 && N < 64, "vmcnt"); asm volatile("s_waitcnt vmcnt(%0)" ::"n"(N) : "memory"); }
template <int N> __device__ __forceinline__ void waitraw(i32x4 (&R)[8]) {
  static_assert(N >= 0 && N < 64, "vmcnt");
  asm volatile("s_waitcnt vmcnt(%8)" : "+v"(R[0]), "+v"(R[1]), "+v"(R[2]), "+v"(R[3]), "+v"(R[4]), "+v"(R[5]), "+v"(R[6]), "+v"(R[7]) : "n"(N));
}
__device__ __forceinline__ void waitl0() { asm volatile("s_waitcnt lgkmcnt(0)" ::: "memory"); }

__global__ __launch_bounds__(512, 2) void k_fused(const unsigned short* __restrict__ x, const int* __restrict__ qw,
                                                  const unsigned short* __restrict__ cb, const unsigned short* __restrict__ absmax,
                                                  unsigned short* __restrict__ out
) {
  extern __shared__ __attribute__((aligned(16))) char smem[];
  LAS char* lds = (LAS char*)smem;
  const int bid = blockIdx.x, xcd = bid & 7, idx = bid >> 3;
  const int tid = threadIdx.x, lane = tid & 63;
  const int wid = __builtin_amdgcn_readfirstlane(tid >> 6);
  if (idx >= 28) {
    const int j = idx - 28;
    const i32x4 srdq = make_srd(qw, (unsigned)(K_ * N_ * 4)), srdx = make_srd(x, (unsigned)(M_ * K_ * 2));
    const int pq = (xcd & 1) * 4 + j;
    unsigned vq[4];
#pragma unroll
    for (int i = 0; i < 4; ++i) {
      const int c = (wid + 8 * i) * 64 + lane, r = c / 224, cc = c % 224;
      vq[i] = (unsigned)((pq * 8 + r) * (N_ * 4) + (xcd >> 1) * 3584 + cc * 16) | ((unsigned)(c >= 1792) << 31);
    }
    const int px = (xcd >> 1) * 4 + j;
    const unsigned vx = (unsigned)(((xcd & 1) * 1024 + px * 64 + wid * 8 + (lane >> 3)) * (K_ * 2) + (lane & 7) * 16);
    i32x4 d[5] = {};
#pragma unroll 1
    for (int st = PFW; st < NS; ++st) {
      const unsigned sq = (unsigned)(st * 64 * N_ * 4), sx = (unsigned)(st * 128);
      waitv<PFD * 5>();
      asm volatile("buffer_load_dwordx4 %0, %1, %2, %3 offen" : "+v"(d[4]) : "v"(vx), "s"(srdx), "s"(sx));
#pragma unroll
      for (int i = 0; i < 4; ++i) asm volatile("buffer_load_dwordx4 %0, %1, %2, %3 offen" : "+v"(d[i]) : "v"(vq[i]), "s"(srdq), "s"(sq));
    }
    waitv<0>();
#pragma unroll
    for (int i = 0; i < 5; ++i) asm volatile("" ::"v"(d[i]));
    return;
  }
  const int mb = (xcd & 1) * 4 + (idx & 3), jb = (xcd >> 1) * 7 + (idx >> 2);
  STAMP(wid >= 4 ? 8 : 0);
  if (wid >= 4) {
    const int L = wid - 4;
    const i32x4 srdq = make_srd(qw, (unsigned)(K_ * N_ * 4));
    const i32x4 srdx = make_srd(x, (unsigned)(M_ * K_ * 2));
    const int c4 = lane & 31, kg = 2 * L + (lane >> 5);
    unsigned vq[8];
    vq[0] = (unsigned)((kg * 8 * N_ + jb * BN + c4 * 4) * 4);
#pragma unroll
    for (int r = 1; r < 8; ++r) vq[r] = vq[0] + r * (N_ * 4);
    i32x4 rawA[8], rawB[8];
    {
      const unsigned s0 = 0;
#pragma unroll
      for (int r = 0; r < 8; ++r) QLOAD(rawA[r], vq[r], srdq, s0);
    }
    SCHED();
    unsigned vx[4];
#pragma unroll
    for (int i = 0; i < 4; ++i) {
      const int row = L * 64 + i * 8 + (lane >> 3);
      const int c = (lane & 7) ^ ((row >> 1) & 7);
      vx[i] = (unsigned)((mb * BM + row) * (K_ * 2) + c * 16 - i * 1024);
    }
#define XPIECE(j, sa_, sb_) do { \
      if ((j) == 0) DMA16(vx[0], srdx, sa_, 0); if ((j) == 1) DMA16(vx[1], srdx, sa_, 1024); if ((j) == 2) DMA16(vx[2], srdx, sa_, 2048); if ((j) == 3) DMA16(vx[3], srdx, sa_, 3072); \
      if ((j) == 4) DMA16(vx[0], srdx, sb_, 0); if ((j) == 5) DMA16(vx[1], srdx, sb_, 1024); if ((j) == 6) DMA16(vx[2], srdx, sb_, 2048); if ((j) == 7) DMA16(vx[3], srdx, sb_, 3072); } while (0)
#define XSTAGE(st_, m_) do { const unsigned sa_ = (unsigned)((st_) * 128), sb_ = sa_ + 32 * K_ * 2, m0_ = (m_), m1_ = m0_ + 4096; \
      SETM0(m0_); NOP0(); XPIECE(0, sa_, sb_); XPIECE(1, sa_, sb_); XPIECE(2, sa_, sb_); XPIECE(3, sa_, sb_); \
      SETM0(m1_); NOP0(); XPIECE(4, sa_, sb_); XPIECE(5, sa_, sb_); XPIECE(6, sa_, sb_); XPIECE(7, sa_, sb_); } while (0)
    unsigned xm = (unsigned)(LDS_X + L * 8192);
    XSTAGE(0, xm);
    {
      const unsigned s1 = 64 * N_ * 4;
#pragma unroll
      for (int r = 0; r < 8; ++r) QLOAD(rawB[r], vq[r], srdq, s1);
    }
    XSTAGE(1, xm + XSLOT);
    SCHED();
    xm += XSLOT;
    const int tile = 2 * (c4 >> 3) + (c4 & 1), a = (c4 & 7) >> 1;
    const int v2 = (tile & 1) + 2 * (a >> 1);
    LAS char* wd[4];
#pragma unroll
    for (int i = 0; i < 4; ++i) wd[i] = lds + LDS_W + (kg >> 2) * 8192 + tile * 1024 + (kg & 3) * 256 + a * 64 + ((i ^ v2) * 16);
    LAS char* tcur = lds + LDS_T + c4 * 16;
    u32x4 tl[4], th[4];
    unsigned pk[4][2];

#define PK(R, i) do { pk[i][0] = perm(R[1][i], R[0][i], 0x0c0c0400u) | perm(R[3][i], R[2][i], 0x04000c0cu); \
                      pk[i][1] = perm(R[5][i], R[4][i], 0x0c0c0400u) | perm(R[7][i], R[6][i], 0x04000c0cu); } while (0)
#define TABLES() do { asm volatile("" : "+v"(tcur)); \
      _Pragma("unroll") for (int i = 0; i < 4; ++i) { tl[i] = *(const LAS u32x4*)(tcur + (2 * i) * 512); th[i] = *(const LAS u32x4*)(tcur + (2 * i + 1) * 512); } \
      tcur += TAB_G; } while (0)
#define LA(i, h) do { c1 = pk[i][h] ^ 0x08080808u; lo = perm(tl[i][1], tl[i][0], pk[i][h]) ^ perm(tl[i][3], tl[i][2], c1); } while (0)
#define LB(i, h) do { const unsigned hi = perm(th[i][1], th[i][0], pk[i][h]) ^ perm(th[i][3], th[i][2], c1); \
                      o[2 * h] = perm(hi, lo, 0x05010400u); o[2 * h + 1] = perm(hi, lo, 0x07030602u); } while (0)
#define WR(i) do { *(LAS u32x4*)(wd[i] + PAR * WSLOT) = o; } while (0)
#define XD(j) do { if (XI) { SCHED(); if ((j) == 4) { SETM0(xm1); NOP0(); } XPIECE(j, sa, sb); SCHED(); } } while (0)
#define QD(r) do { if (RAW) { SCHED(); QLOAD(R[r], vq[r], srdq, sqn); SCHED(); } } while (0)

    BAR();
    __builtin_amdgcn_s_setprio(1);
    STAMP(9);
    TABLES();
    waitraw<24>(rawA);
    PK(rawA, 0); PK(rawA, 1); PK(rawA, 2); PK(rawA, 3);
    int st = 0;
    auto iter = [&](auto PARc, auto XIc, auto RAWc, auto NXTc, auto WNc, auto WXc, i32x4 (&R)[8], i32x4 (&RN)[8]) {
      constexpr int PAR = decltype(PARc)::value, WN = decltype(WNc)::value, WX = decltype(WXc)::value;
      constexpr bool XI = decltype(XIc)::value, RAW = decltype(RAWc)::value, NXT = decltype(NXTc)::value;
      const unsigned sa = (unsigned)((st + 1) * 128), sb = sa + 32 * K_ * 2, xm1 = xm + 4096;
      const unsigned sqn = (unsigned)((st + 2) * 64 * N_ * 4);
      unsigned c1, lo; u32x4 o;
      XD(0); LA(0, 0); LB(0, 0); QD(0); LA(0, 1); XD(1); LB(0, 1); WR(0);
      QD(1); LA(1, 0); LB(1, 0); XD(2); LA(1, 1); QD(2); LB(1, 1); WR(1);
      XD(3); LA(2, 0); LB(2, 0); QD(3); LA(2, 1); XD(4); LB(2, 1); WR(2);
      QD(4); LA(3, 0); LB(3, 0); XD(5); LA(3, 1); QD(5); LB(3, 1); WR(3);
      SCHED();
      if (NXT) {
        if (PAR == 1) TABLES();
        waitraw<WN>(RN);
        XD(6); PK(RN, 0); QD(6); PK(RN, 1); XD(7); PK(RN, 2); QD(7); PK(RN, 3);
      }
      SCHED();
      ++st;
      xm = (xm >= (unsigned)(LDS_X + 2 * XSLOT)) ? xm - 2 * XSLOT : xm + XSLOT;
      SETM0(xm);
      waitv<WX>();
      waitl0();
      BAR();
    };
    iter(IC(0), IC(0), IC(1), IC(1), IC(8 + 6), IC(8 + 8 + 8), rawA, rawB);
    iter(IC(1), IC(1), IC(1), IC(1), IC(12), IC(8 + 16), rawB, rawA);
#pragma unroll 1
    for (int g = 1; g < 7; ++g) {
      iter(IC(0), IC(1), IC(1), IC(1), IC(12), IC(17), rawA, rawB);
      iter(IC(1), IC(1), IC(1), IC(1), IC(12), IC(17), rawB, rawA);
    }
    iter(IC(0), IC(1), IC(0), IC(1), IC(6), IC(9), rawA, rawB);
    iter(IC(1), IC(0), IC(0), IC(0), IC(0), IC(0), rawB, rawA);
    STAMP(11);
  } else {
    unsigned am[4];
    {
      const i32x4 srda = make_srd(absmax, (unsigned)(8 * N_ * 2));
#pragma unroll
      for (int i = 0; i < 4; ++i) {
        const unsigned vb = (unsigned)(((2 * i + (tid >> 7)) * N_ + jb * BN + (tid & 127)) * 2), z = 0;
        asm volatile("buffer_load_ushort %0, %1, %2, %3 offen" : "=v"(am[i]) : "v"(vb), "s"(srda), "s"(z));
      }
    }
    const u32x4 cbr0 = *(const u32x4*)cb, cbr1 = *(const u32x4*)(cb + 8);
    f32x4 acc[4][8];
#pragma unroll
    for (int m = 0; m < 4; ++m)
#pragma unroll
      for (int t = 0; t < 8; ++t) { acc[m][t] = f32x4{0.f, 0.f, 0.f, 0.f}; asm volatile("" : "+v"(acc[m][t])); }
    float cf[16];
#pragma unroll
    for (int j = 0; j < 4; ++j) { cf[2 * j] = __uint_as_float(cbr0[j] << 16); cf[2 * j + 1] = __uint_as_float(cbr0[j] & 0xffff0000u);
                                  cf[8 + 2 * j] = __uint_as_float(cbr1[j] << 16); cf[8 + 2 * j + 1] = __uint_as_float(cbr1[j] & 0xffff0000u); }
    auto plane = [](u32x4 t) {
      const unsigned a1 = t[1] ^ 0xFFFFFF00u, b1 = t[3] ^ 0xFFFFFF00u;
      unsigned a0 = t[0], b0 = t[2];
#pragma unroll
      for (int it = 0; it < 3; ++it) { const unsigned na = t[0] ^ perm(b1, b0, 0x0B0A0908u), nb = t[2] ^ perm(a1, a0, 0x0B0A0908u); a0 = na; b0 = nb; }
      return u32x4{a0, a1, b0, b1};
    };
    LAS char* tw = lds + LDS_T + (tid >> 7) * TAB_G + (tid & 3) * 1024 + ((tid & 127) >> 2) * 16;
    auto build = [&](unsigned amv) {
      const float s = __uint_as_float(amv << 16);
      unsigned d[8];
#pragma unroll
      for (int j = 0; j < 8; ++j) d[j] = cvt_pk_bf16(cf[2 * j] * s, cf[2 * j + 1] * s);
      u32x4 lo, hi;
#pragma unroll
      for (int j = 0; j < 4; ++j) { lo[j] = perm(d[2 * j + 1], d[2 * j], 0x06040200u); hi[j] = perm(d[2 * j + 1], d[2 * j], 0x07050301u); }
      *(LAS u32x4*)(tw) = plane(lo);
      *(LAS u32x4*)(tw + 512) = plane(hi);
      tw += 2 * TAB_G;
    };
    asm volatile("s_waitcnt vmcnt(0)" : "+v"(am[0]), "+v"(am[1]), "+v"(am[2]), "+v"(am[3]));
    build(am[0]);
    waitl0();
    STAMP(7);
    BAR();
    const int fr = lane & 15, fq = lane >> 4;
    const int xi0 = LDS_X + wid * 64 * 128 + (fr >> 3) * 1024 + (fr & 7) * 128 + ((fq ^ ((fr >> 1) & 7)) * 16);
    LAS char* const xa0 = lds + xi0;
    LAS char* const xa1 = lds + (xi0 ^ 64);
    LAS char* const wae = lds + LDS_W + fq * 256 + (fr ^ (2 * (fr >> 3))) * 16;
    LAS char* const wao = lds + LDS_W + fq * 256 + (fr ^ (1 + 2 * (fr >> 3))) * 16;
    bf16x8 xf0[4], wf0[8], xf1[4], wf1[8];
#define RD(p) (*(const LAS bf16x8*)(p))
#define LOADF(xf, wf, xa, we, wo_, wofs) do { _Pragma("unroll") for (int t = 0; t < 8; ++t) wf[t] = RD(((t & 1) ? (wo_) : (we)) + (wofs) + t * 1024); \
                                              _Pragma("unroll") for (int m = 0; m < 4; ++m) xf[m] = RD((xa) + m * 2048); } while (0)
#define MMA(xf, wf) do { _Pragma("unroll") for (int m = 0; m < 4; ++m) _Pragma("unroll") for (int t = 0; t < 8; ++t) \
      acc[m][t] = __builtin_amdgcn_mfma_f32_16x16x32_bf16(wf[t], xf[m], acc[m][t], 0, 0, 0); } while (0)
#define INTERLEAVE() do { _Pragma("unroll") for (int i = 0; i < 12; ++i) { __builtin_amdgcn_sched_group_barrier(0x8, 1, 0); __builtin_amdgcn_sched_group_barrier(0x100, 1, 0); } \
      __builtin_amdgcn_sched_group_barrier(0x8, 20, 0); } while (0)
    BAR();
    STAMP(2);
    LOADF(xf0, wf0, xa0, wae, wao, 0);
    constexpr unsigned XSEQ = [] { unsigned v = 0; for (int s = 0; s < 16; ++s) v |= (unsigned)(s % 3) << (2 * s); return v; }();
    LAS char *a1 = xa1, *e1 = wae, *o1 = wao;
    auto stage = [&](int s, auto TABc) {
      const int xo = (int)((XSEQ >> (2 * s + 2)) & 3) * XSLOT, wo = ((s + 1) & 1) * WSLOT;
      LAS char *a0n = xa0 + xo, *a1n = xa1 + xo, *e1n = wae + wo, *o1n = wao + wo;
      LOADF(xf1, wf1, a1, e1, o1, 8192);
      MMA(xf0, wf0);
      INTERLEAVE();
      LAS char* a0 = a0n; a1 = a1n; e1 = e1n; o1 = o1n;
      asm volatile("" : "+v"(a0), "+v"(a1), "+v"(e1), "+v"(o1));
      SCHED();
      if (decltype(TABc)::value) {
        build(am[1]);
        am[1] = am[2]; am[2] = am[3];
        SCHED();
      }
      waitl0();
      BAR();
      SCHED();
      LOADF(xf0, wf0, a0, e1, o1, 0);
      MMA(xf1, wf1);
      INTERLEAVE();
      SCHED();
    };
#pragma unroll 1
    for (int s = 0; s < 3; ++s) stage(s, IC(1));
#pragma unroll 1
    for (int s = 3; s < NS - 1; ++s) stage(s, IC(0));
    LOADF(xf1, wf1, a1, e1, o1, 8192);
    MMA(xf0, wf0);
    INTERLEAVE();
    MMA(xf1, wf1);
    {
      LAS char* const sb = lds + LDS_X + XSLOT + wid * 16384;
      LAS char* const wa_ = sb + fr * 256;
      const int r4 = lane >> 4, c16 = lane & 15;
      const unsigned vo = (unsigned)(((mb * BM + wid * 64 + r4) * N_ + jb * BN) * 2 + c16 * 16);
      const __amdgpu_buffer_rsrc_t rs = __builtin_amdgcn_make_buffer_rsrc((void*)out, 0, M_ * N_ * 2, 0x00020000);
#pragma unroll
      for (int m = 0; m < 4; ++m) {
#pragma unroll
        for (int pp = 0; pp < 4; ++pp) {
          u32x4 o;
          o.x = cvt_pk_bf16(acc[m][2 * pp][0], acc[m][2 * pp][1]); o.y = cvt_pk_bf16(acc[m][2 * pp][2], acc[m][2 * pp][3]);
          o.z = cvt_pk_bf16(acc[m][2 * pp + 1][0], acc[m][2 * pp + 1][1]); o.w = cvt_pk_bf16(acc[m][2 * pp + 1][2], acc[m][2 * pp + 1][3]);
          *(LAS u32x4*)(wa_ + m * 4096 + (((4 * pp + fq) ^ fr) * 16)) = o;
        }
        u32x4 v[4];
#pragma unroll
        for (int i = 0; i < 4; ++i) v[i] = *(const LAS u32x4*)(sb + m * 4096 + (4 * i + r4) * 256 + ((c16 ^ (4 * i + r4)) * 16));
#pragma unroll
        for (int i = 0; i < 4; ++i) __builtin_amdgcn_raw_buffer_store_b128(v[i], rs, vo, (m * 4 + i) * 4 * N_ * 2, STPOL);
      }
    }
  }
}

extern "C" void kernel_launch(void* const* d_in, const int* in_sizes, int n_in, void* d_out, int out_size, void* d_ws,
                              size_t ws_size, hipStream_t stream) {
  const unsigned short* x = (const unsigned short*)d_in[0];
  const int* qw = (const int*)d_in[1];
  const unsigned short* cb = (const unsigned short*)d_in[2];
  const unsigned short* absmax = (const unsigned short*)d_in[3];
  unsigned short* out = (unsigned short*)d_out;
  (void)hipFuncSetAttribute((const void*)k_fused, hipFuncAttributeMaxDynamicSharedMemorySize, LDS_BYTES);
  k_fused<<<256, 512, LDS_BYTES, stream>>>(x, qw, cb, absmax, out
  );
}
```
